```python
import torch

def _softmax(x, axis=-1):
    return torch.softmax(x, dim=axis)
def _gelu(x):
    return torch.nn.functional.gelu(x, approximate="tanh")
def _silu(x):
    return torch.nn.functional.silu(x)
def _sigmoid(x):
    return torch.sigmoid(x)
def _layernorm(x, axis=-1, eps=1e-5):
    m = x.mean(dim=axis, keepdim=True)
    v = x.var(dim=axis, keepdim=True, unbiased=False)
    return (x - m) / torch.sqrt(v + eps)
def _scatter_add(tgt, idx, src, axis=0):
    return tgt.clone().index_add_(axis, idx, src)
def _segment_sum(src, seg, n):
    out = torch.zeros((n,) + src.shape[1:], dtype=src.dtype)
    return out.index_add_(0, seg, src)
def _pad(x, pw):
    flat = tuple(v for pair in reversed(pw) for v in pair)
    return torch.nn.functional.pad(x, flat)

kernel_name = 'concat_heads__linear_kernel_hedgehog__ssm_conv_s5__prompt'

def setup_inputs():

    _w = (lambda a: (lambda t: t.to(torch.bfloat16) if t.is_floating_point() else t)(torch.as_tensor(a)))
    def _A_setup():
        B, S, D, H = 1, 2048, 1024, 32
        Dh = D // H
        return {
            "x": _w((lambda *s: torch.randn(*s, dtype=torch.bfloat16) * 0.4)(B, S, D)),
            "wqkv": _w((lambda *s: torch.randn(*s, dtype=torch.bfloat16) * 0.4)(D, 3 * D)),
            "w_phi": _w((lambda *s: torch.randn(*s, dtype=torch.bfloat16) * 0.4)(H, Dh, Dh) * (Dh ** -0.5)),
            "wo": _w((lambda *s: torch.randn(*s, dtype=torch.bfloat16) * 0.05)(D, D)),
        }
    _A_in = _A_setup()

    def _B_setup():
        B, S, D = 1, 2048, 1024
        P = D
        Lambda_re = -0.5 * (lambda _x: torch.exp(torch.as_tensor(_x)))((lambda *s: torch.randn(*s, dtype=torch.bfloat16) * 0.8)(P))
        Lambda_im = (lambda *s: torch.randn(*s, dtype=torch.bfloat16) * 0.8)(P) * 1.0
        B_re = (lambda *s: torch.randn(*s, dtype=torch.bfloat16) * 0.8)(P, D) * (D ** -0.5)
        B_im = (lambda *s: torch.randn(*s, dtype=torch.bfloat16) * 0.8)(P, D) * (D ** -0.5)
        C_re = (lambda *s: torch.randn(*s, dtype=torch.bfloat16) * 0.8)(D, P) * (P ** -0.5)
        C_im = (lambda *s: torch.randn(*s, dtype=torch.bfloat16) * 0.8)(D, P) * (P ** -0.5)
        log_dt = (lambda *s: torch.randn(*s, dtype=torch.bfloat16) * 0.8)(P) * 0.3 - 2.0
        Dskip = (lambda *s: torch.randn(*s, dtype=torch.bfloat16) * 0.8)(D)
        return {
            "x": _w((lambda *s: torch.randn(*s, dtype=torch.bfloat16) * 0.05)(B, S, D)),
            "Lambda_re": _w(Lambda_re),
            "Lambda_im": _w(Lambda_im),
            "B_re": _w(B_re),
            "B_im": _w(B_im),
            "C_re": _w(C_re),
            "C_im": _w(C_im),
            "log_dt": _w(log_dt),
            "Dskip": _w(Dskip),
        }
    _B_in = _B_setup()

    return {
        "A_x": _A_in["x"],
        "A_wqkv": _A_in["wqkv"],
        "A_w_phi": _A_in["w_phi"],
        "A_wo": _A_in["wo"],
        "B_x": _B_in["x"],
        "B_Lambda_re": _B_in["Lambda_re"],
        "B_Lambda_im": _B_in["Lambda_im"],
        "B_B_re": _B_in["B_re"],
        "B_B_im": _B_in["B_im"],
        "B_C_re": _B_in["C_re"],
        "B_C_im": _B_in["C_im"],
        "B_log_dt": _B_in["log_dt"],
        "B_Dskip": _B_in["Dskip"],
    }

def reference(A_x, A_wqkv, A_w_phi, A_wo, B_x, B_Lambda_re, B_Lambda_im, B_B_re, B_B_im, B_C_re, B_C_im, B_log_dt, B_Dskip):
    def _A_ref(x, wqkv, w_phi, wo):
        B, S, D = x.shape
        H = 32; Dh = D // H
        qkv = (lambda x, *d: x.permute(*d))((x @ wqkv).reshape(B, S, 3, H, Dh), 2, 0, 3, 1, 4)
        q, k, v = qkv[0], qkv[1], qkv[2]
        pq = torch.einsum("bhsd,hdf->bhsf", q, w_phi)
        pk = torch.einsum("bhsd,hdf->bhsf", k, w_phi)
        phi_q = torch.concatenate([_softmax(pq, axis=-1), _softmax(-pq, axis=-1)], axis=-1)
        phi_k = torch.concatenate([_softmax(pk, axis=-1), _softmax(-pk, axis=-1)], axis=-1)
        kv = torch.einsum("bhsf,bhsd->bhfd", phi_k, v)
        z  = torch.einsum("bhsf->bhf", phi_k)
        num = torch.einsum("bhqf,bhfd->bhqd", phi_q, kv)
        den = torch.einsum("bhqf,bhf->bhq", phi_q, z)[..., None] + 1e-6
        o = num / den
        return (lambda x, *d: x.permute(*d))(o, 0, 2, 1, 3).reshape(B, S, D) @ wo

    def _B_ref(x, Lambda_re, Lambda_im, B_re, B_im, C_re, C_im, log_dt, Dskip):
        Bsz, L, H = x.shape
        Lam = Lambda_re + 1j * Lambda_im
        Bmat = B_re + 1j * B_im
        Cmat = C_re + 1j * C_im
        dt = torch.exp(log_dt)
        Lam_bar = torch.exp(dt * Lam)
        B_bar = ((Lam_bar - 1.0) / Lam)[:, None] * Bmat
        Bu = torch.einsum("bld,pd->blp", x + 0j, B_bar)
        h = torch.zeros_like(Bu[:, 0, :])
        hs = []
        for t in range(L):
            h = Lam_bar[None, :] * h + Bu[:, t, :]
            hs.append(h)
        hseq = torch.stack(hs, 1)
        y = 2.0 * torch.einsum("blp,dp->bld", hseq, Cmat).real + x * Dskip[None, None, :]
        return y

    a = _A_ref(A_x, A_wqkv, A_w_phi, A_wo)
    b = _B_ref(B_x, B_Lambda_re, B_Lambda_im, B_B_re, B_B_im, B_C_re, B_C_im, B_log_dt, B_Dskip)
    return torch.concatenate([a, b], axis=-1)
```

```cpp
#include <hip/hip_runtime.h>
#include <hip/hip_bf16.h>
typedef unsigned short u16;
__device__ __forceinline__ float bf2f(u16 v) { return __uint_as_float(((unsigned)v) << 16); }
__device__ __forceinline__ float rbf(float f) { unsigned u = __float_as_uint(f); u += 0x7fffu + ((u >> 16) & 1u); return __uint_as_float(u & 0xffff0000u); }

#define S_ 2048
#define D_ 1024
#define H_ 32

__global__ void n_gemm_kn(const u16* A, const u16* B, float* C, int M, int N, int K, int ldc, int round_bf) {
  int n = blockIdx.x * blockDim.x + threadIdx.x, m = blockIdx.y;
  if (n >= N) return;
  float s = 0.f;
  for (int k = 0; k < K; ++k) s += bf2f(A[(size_t)m * K + k]) * bf2f(B[(size_t)k * N + n]);
  C[(size_t)m * ldc + n] = round_bf ? rbf(s) : s;
}
__global__ void n_gemm_fkn(const float* A, const u16* B, float* C, int M, int N, int K, int ldc, int round_bf) {
  int n = blockIdx.x * blockDim.x + threadIdx.x, m = blockIdx.y;
  if (n >= N) return;
  float s = 0.f;
  for (int k = 0; k < K; ++k) s += A[(size_t)m * K + k] * bf2f(B[(size_t)k * N + n]);
  C[(size_t)m * ldc + n] = round_bf ? rbf(s) : s;
}
__global__ void n_gemm_nk(const u16* A, const u16* Bt, float* C, int M, int N, int K) {
  int n = blockIdx.x * blockDim.x + threadIdx.x, m = blockIdx.y;
  if (n >= N) return;
  float s = 0.f;
  for (int k = 0; k < K; ++k) s += bf2f(A[(size_t)m * K + k]) * bf2f(Bt[(size_t)n * K + k]);
  C[(size_t)m * N + n] = s;
}
__global__ void n_phi(const float* qkv, const u16* wphi, float* phi) {
  int idx = blockIdx.x * blockDim.x + threadIdx.x;
  if (idx >= 2 * S_ * H_) return;
  int which = idx / (S_ * H_), r = idx % (S_ * H_), s = r / H_, h = r % H_;
  const float* q = qkv + (size_t)s * 3072 + which * 1024 + h * 32;
  float* o = phi + (size_t)idx * 64;
  float mx = -1e30f, mn = 1e30f;
  for (int f = 0; f < 32; ++f) {
    float a = 0.f;
    for (int d = 0; d < 32; ++d) a += q[d] * bf2f(wphi[(h * 32 + d) * 32 + f]);
    o[f] = a; mx = fmaxf(mx, a); mn = fminf(mn, a);
  }
  float s1 = 0.f, s2 = 0.f;
  for (int f = 0; f < 32; ++f) { float a = o[f]; float e1 = expf(a - mx), e2 = expf(mn - a); o[f] = e1; o[32 + f] = e2; s1 += e1; s2 += e2; }
  for (int f = 0; f < 32; ++f) { o[f] = o[f] / s1; o[32 + f] = o[32 + f] / s2; }
}
__global__ void n_kv(const float* qkv, const float* phi, float* kv) {
  int idx = blockIdx.x * blockDim.x + threadIdx.x;
  if (idx >= H_ * 64 * 33) return;
  int h = idx / (64 * 33), r = idx % (64 * 33), f = r / 33, d = r % 33;
  const float* pk = phi + (size_t)S_ * H_ * 64;
  float a = 0.f;
  for (int s = 0; s < S_; ++s) {
    float v = d < 32 ? qkv[(size_t)s * 3072 + 2048 + h * 32 + d] : 1.f;
    a += pk[((size_t)s * H_ + h) * 64 + f] * v;
  }
  kv[idx] = a;
}
__global__ void n_o(const float* phi, const float* kv, float* o) {
  int idx = blockIdx.x * blockDim.x + threadIdx.x;
  if (idx >= S_ * D_) return;
  int s = idx / D_, c = idx % D_, h = c / 32, d = c % 32;
  const float* pq = phi + ((size_t)s * H_ + h) * 64;
  float num = 0.f, den = 0.f;
  for (int f = 0; f < 64; ++f) { num += pq[f] * kv[(h * 64 + f) * 33 + d]; den += pq[f] * kv[(h * 64 + f) * 33 + 32]; }
  o[idx] = num / (den + 1e-6f);
}
__global__ void n_scan(const u16* lre, const u16* lim, const u16* logdt, float* ure, float* uim) {
  int p = blockIdx.x * blockDim.x + threadIdx.x;
  if (p >= D_) return;
  float a = bf2f(lre[p]), b = bf2f(lim[p]);
  float dt = rbf(expf(bf2f(logdt[p])));
  float er = expf(dt * a); float lbr = er * cosf(dt * b), lbi = er * sinf(dt * b);
  float nr = lbr - 1.f, ni = lbi, den = a * a + b * b;
  float cr = (nr * a + ni * b) / den, ci = (ni * a - nr * b) / den;
  float hr = 0.f, hi = 0.f;
  for (int t = 0; t < S_; ++t) {
    float xr = ure[(size_t)t * D_ + p], xi = uim[(size_t)t * D_ + p];
    float br = cr * xr - ci * xi, bi = cr * xi + ci * xr;
    float nhr = lbr * hr - lbi * hi + br, nhi = lbr * hi + lbi * hr + bi;
    hr = nhr; hi = nhi;
    ure[(size_t)t * D_ + p] = hr; uim[(size_t)t * D_ + p] = hi;
  }
}
__global__ void n_y(const float* hre, const float* him, const u16* cre, const u16* cim, const u16* x, const u16* dskip, float* out) {
  int d = blockIdx.x * blockDim.x + threadIdx.x, t = blockIdx.y;
  if (d >= D_) return;
  float s = 0.f;
  for (int p = 0; p < D_; ++p) s += hre[(size_t)t * D_ + p] * bf2f(cre[(size_t)d * D_ + p]) - him[(size_t)t * D_ + p] * bf2f(cim[(size_t)d * D_ + p]);
  out[(size_t)t * 2048 + 1024 + d] = 2.f * s + rbf(bf2f(x[(size_t)t * D_ + d]) * bf2f(dskip[d]));
}

#define NAIVE_NAME kernel_launch
extern "C" void NAIVE_NAME(void* const* d_in, const int* in_sizes, int n_in,
                              void* d_out, int out_size, void* d_ws, size_t ws_size,
                              hipStream_t stream) {
  const u16* Ax = (const u16*)d_in[0]; const u16* wqkv = (const u16*)d_in[1]; const u16* wphi = (const u16*)d_in[2]; const u16* wo = (const u16*)d_in[3];
  const u16* Bx = (const u16*)d_in[4]; const u16* lre = (const u16*)d_in[5]; const u16* lim = (const u16*)d_in[6];
  const u16* bre = (const u16*)d_in[7]; const u16* bim = (const u16*)d_in[8]; const u16* cre = (const u16*)d_in[9]; const u16* cim = (const u16*)d_in[10];
  const u16* logdt = (const u16*)d_in[11]; const u16* dskip = (const u16*)d_in[12];
  float* out = (float*)d_out;
  float* ws = (float*)d_ws;
  float* qkv = ws;
  float* phi = qkv + (size_t)S_ * 3072;
  float* kv = phi + (size_t)2 * S_ * H_ * 64;
  float* o = kv + 32 * 64 * 33 + 64;
  float* ure = o + (size_t)S_ * D_;
  float* uim = ure + (size_t)S_ * D_;
  n_gemm_kn<<<dim3(3072 / 256, S_), 256, 0, stream>>>(Ax, wqkv, qkv, S_, 3072, D_, 3072, 1);
  n_phi<<<(2 * S_ * H_ + 255) / 256, 256, 0, stream>>>(qkv, wphi, phi);
  n_kv<<<(H_ * 64 * 33 + 255) / 256, 256, 0, stream>>>(qkv, phi, kv);
  n_o<<<(S_ * D_) / 256, 256, 0, stream>>>(phi, kv, o);
  n_gemm_fkn<<<dim3(1024 / 256, S_), 256, 0, stream>>>(o, wo, out, S_, 1024, D_, 2048, 0);
  n_gemm_nk<<<dim3(1024 / 256, S_), 256, 0, stream>>>(Bx, bre, ure, S_, D_, D_);
  n_gemm_nk<<<dim3(1024 / 256, S_), 256, 0, stream>>>(Bx, bim, uim, S_, D_, D_);
  n_scan<<<D_ / 64, 64, 0, stream>>>(lre, lim, logdt, ure, uim);
  n_y<<<dim3(1024 / 256, S_), 256, 0, stream>>>(ure, uim, cre, cim, Bx, dskip, out);
}
```

```cpp
#include <hip/hip_runtime.h>
#include <hip/hip_bf16.h>

typedef unsigned short u16;
typedef unsigned int u32;
typedef unsigned long long u64;
typedef __bf16 bf16x8 __attribute__((ext_vector_type(8)));
typedef __bf16 bf16x2 __attribute__((ext_vector_type(2)));
typedef short s16x4 __attribute__((ext_vector_type(4)));
typedef short s16x8 __attribute__((ext_vector_type(8)));
typedef float f32x16 __attribute__((ext_vector_type(16)));
typedef float f32x4 __attribute__((ext_vector_type(4)));
typedef float f32x2 __attribute__((ext_vector_type(2)));
typedef u32 u32x4 __attribute__((ext_vector_type(4)));
typedef u32 u32x2 __attribute__((ext_vector_type(2)));
#define LAS __attribute__((address_space(3)))
#define GAS __attribute__((address_space(1)))
#define DI __device__ __forceinline__
#define RLX_AGENT __ATOMIC_RELAXED, __HIP_MEMORY_SCOPE_AGENT

constexpr size_t WS_EPOCH_A = 0;
constexpr size_t WS_EPOCH_B = 256;
constexpr size_t WS_KVFLAG = 4096;
constexpr size_t WS_OFLAG = WS_KVFLAG + 16 * 128;
constexpr size_t WS_HFLAG = WS_OFLAG + 8 * 128;
constexpr size_t WS_AGG = 65536;
constexpr size_t WS_KVP = WS_AGG + 16 * 8 * 1024;
constexpr size_t KVP_BYTES = 2 * (2048 + 64) * 4;
constexpr size_t WS_O = 4u << 20;
constexpr size_t WS_H = 8u << 20;
constexpr size_t WS_END = 16u << 20;

constexpr int LDS_BYTES = 163840;

struct Params {
  const u16* Ax; const u16* wqkv; const u16* wphi; const u16* wo;
  const u16* Bx; const u16* lre; const u16* lim; const u16* bre; const u16* bim;
  const u16* cre; const u16* cim; const u16* logdt; const u16* dskip;
  float* out; char* ws;
};

DI float bf2f(u16 v) { return __uint_as_float(((u32)v) << 16); }
DI u32 pk2(float a, float b) {
  f32x2 v = {a, b};
  bf16x2 r = __builtin_convertvector(v, bf16x2);
  return __builtin_bit_cast(u32, r);
}
DI bf16x8 pack8(const f32x16& x, int s) {
  u32x4 p;
  p[0] = pk2(x[8 * s + 0], x[8 * s + 1]); p[1] = pk2(x[8 * s + 2], x[8 * s + 3]);
  p[2] = pk2(x[8 * s + 4], x[8 * s + 5]); p[3] = pk2(x[8 * s + 6], x[8 * s + 7]);
  return __builtin_bit_cast(bf16x8, p);
}
#define MFMA(a, b, c) __builtin_amdgcn_mfma_f32_32x32x16_bf16((a), (b), (c), 0, 0, 0)
#define LMFMA MFMA
DI bf16x8 lds_frag(LAS char* p) { return *(LAS bf16x8*)p; }
DI s16x4 lds_tr(LAS char* __restrict__ p) { return __builtin_amdgcn_ds_read_tr16_b64_v4i16((LAS s16x4*)p); }
DI bf16x8 lds_frag_tr(LAS char* p, int off2) {
  s16x4 lo = lds_tr(p), hi = lds_tr(p + off2);
  s16x8 v = __builtin_shufflevector(lo, hi, 0, 1, 2, 3, 4, 5, 6, 7);
  return __builtin_bit_cast(bf16x8, v);
}
DI void dma16(const char* g, LAS char* l) {
  __builtin_amdgcn_global_load_lds((const GAS u32*)g, (LAS u32*)l, 16, 0, 0);
}
template <int N> DI void wait_vm() { asm volatile("s_waitcnt vmcnt(%0)" ::"n"(N) : "memory"); }
DI void wait_lgkm0() { asm volatile("" ::: "memory"); __builtin_amdgcn_s_waitcnt(0xC07F); asm volatile("" ::: "memory"); }
#define SB() __builtin_amdgcn_sched_barrier(0)
DI void bar() { asm volatile("" ::: "memory"); __builtin_amdgcn_s_barrier(); asm volatile("" ::: "memory"); }
DI float xhalf_max(float v) {
  auto r = __builtin_amdgcn_permlane32_swap(__float_as_uint(v), __float_as_uint(v), false, false);
  return fmaxf(__uint_as_float(r[0]), __uint_as_float(r[1]));
}
DI float xhalf_min(float v) {
  auto r = __builtin_amdgcn_permlane32_swap(__float_as_uint(v), __float_as_uint(v), false, false);
  return fminf(__uint_as_float(r[0]), __uint_as_float(r[1]));
}
DI float xhalf_sum(float v) {
  auto r = __builtin_amdgcn_permlane32_swap(__float_as_uint(v), __float_as_uint(v), false, false);
  return __uint_as_float(r[0]) + __uint_as_float(r[1]);
}
DI u64 next_tag(u64 e) { return e * 6364136223846793005ull + 1442695040888963407ull; }

DI void wait_flags(const char* flags, int n, u64 tag, int lane) {
  const GAS u64* f = (const GAS u64*)flags + (lane < n ? lane : 0);
  const u64 t0 = __builtin_amdgcn_s_memrealtime();
  for (;;) {
    u64 v = __hip_atomic_load(f, RLX_AGENT);
    if (__all(v == tag)) break;
    if (__builtin_amdgcn_s_memrealtime() - t0 > 500000ull) break;
    __builtin_amdgcn_s_sleep(1);
  }
}

DI void dual_softmax(const f32x16& p, f32x16& ep, f32x16& em) {
  float mx = p[0], mn = p[0];
#pragma unroll
  for (int i = 1; i < 16; ++i) { mx = fmaxf(mx, p[i]); mn = fminf(mn, p[i]); }
  mx = xhalf_max(mx); mn = xhalf_min(mn);
  const float L2E = 1.4426950408889634f;
  const float bp = -mx * L2E, bm = mn * L2E;
  float sp = 0.f, sm = 0.f;
#pragma unroll
  for (int i = 0; i < 16; ++i) {
    ep[i] = __builtin_amdgcn_exp2f(fmaf(p[i], L2E, bp));
    em[i] = __builtin_amdgcn_exp2f(fmaf(p[i], -L2E, bm));
    sp += ep[i]; sm += em[i];
  }
  sp = xhalf_sum(sp); sm = xhalf_sum(sm);
  const float rp = 1.0f / sp, rm = 1.0f / sm;
#pragma unroll
  for (int i = 0; i < 16; ++i) { ep[i] *= rp; em[i] *= rm; }
}

template <int NM, int NV, int ND> DI void interleave() {
  constexpr int DPM = (ND + NM - 2) / (NM - 1);
#pragma unroll
  for (int i = 0; i < NM; ++i) {
    __builtin_amdgcn_sched_group_barrier(0x008, 1, 0);
    if (i * DPM < ND) __builtin_amdgcn_sched_group_barrier(0x100, DPM, 0);
    if (NV > 0 && i < NV) __builtin_amdgcn_sched_group_barrier(0x020, (NV + NM - 1) / NM, 0);
  }
}

template <bool ISA>
DI void p1_gemm(LAS char* lds, const char* xg, const char* wg0, const char* wg1, f32x16 (&acc)[2][ISA ? 3 : 2], int w, int lane) {
  constexpr int NT = ISA ? 3 : 2;
  constexpr int WOFF = 98304, WSLOT = ISA ? 12288 : 16384, NW = ISA ? 5 : 3;
  constexpr int NM = 2 * NT, ND = ISA ? 8 : 4;
  const int wm = w >> 1, wn = w & 1, h = lane >> 5, rho = lane & 31;
  const u32 xoffE = (u32)((32 * w + (lane >> 3)) * 2048 + (((lane & 7) ^ (lane >> 4)) * 16));
  const u32 xoffO = (u32)((32 * w + 8 + (lane >> 3)) * 2048 + (((lane & 7) ^ (4 + (lane >> 4))) * 16));
  u32 woff0, woff1;
  const char* wbase;
  int wlds;
  if constexpr (ISA) {
    const int ib0 = w < 4 ? 2 * w : 4 + w;
    const int c = (lane & 7) ^ (((lane >> 4) & 1) << 2);
    woff0 = (u32)((8 * (ib0 & 3) + (lane >> 3)) * 6144 + (ib0 >> 2) * 2048 + c * 16);
    woff1 = woff0 + 8 * 6144;
    wbase = wg0;
    wlds = WOFF + ib0 * 1024;
  } else {
    woff0 = (u32)((16 * (w & 3) + (lane >> 3)) * 2048 + (((lane & 7) ^ (lane >> 4)) * 16));
    woff1 = (u32)((16 * (w & 3) + 8 + (lane >> 3)) * 2048 + (((lane & 7) ^ (4 + (lane >> 4))) * 16));
    wbase = w < 4 ? wg0 : wg1;
    wlds = WOFF + w * 2048;
  }
  auto issue_x = [&](int T, int so) {
    LAS char* s = lds + so + w * 4096;
    const char* xs = xg + T * 128;
    dma16(xs + xoffE, s); dma16(xs + xoffO, s + 1024);
    dma16(xs + xoffE + 16 * 2048, s + 2048); dma16(xs + xoffO + 16 * 2048, s + 3072);
  };
  auto issue_w = [&](int u, int so) {
    LAS char* s = lds + so + wlds;
    if constexpr (ISA) {
      const char* ws_ = wbase + (size_t)u * (32 * 6144);
      dma16(ws_ + woff0, s);
      if (w < 4) dma16(ws_ + woff1, s + 1024);
    } else {
      dma16(wbase + u * 128 + woff0, s); dma16(wbase + u * 128 + woff1, s + 1024);
    }
  };
  const int arow = wm * 64 + (ISA ? rho : (32 * ((rho >> 2) & 1) + (rho & 3) + 4 * (rho >> 3)));
  constexpr int AM = ISA ? 32 * 128 : 16 * 128;
  const int aoff = arow * 128 + ((h ^ ((arow >> 1) & 7)) * 16);
  int boff;
  if constexpr (ISA) {
    const int blk = (lane >> 4) & 1, q = (lane & 15) >> 2, p = lane & 3;
    const int c = (4 * wn + 2 * blk + (p >> 1)) ^ (((q >> 1) & 1) << 2);
    boff = WOFF + (8 * h + q) * 128 + c * 16 + 8 * (p & 1);
  } else {
    const int brow = wn * 32 + rho;
    boff = WOFF + brow * 128 + ((h ^ ((brow >> 1) & 7)) * 16);
  }
  bf16x8 fa0[2], fb0[NT], fa1[2], fb1[NT];
  auto rd = [&](bf16x8 (&fa)[2], bf16x8 (&fb)[NT], int xso, int wso, int s4) {
    LAS char* pa = lds + xso + (aoff ^ (s4 * 32));
    fa[0] = lds_frag(pa); fa[1] = lds_frag(pa + AM);
    if constexpr (ISA) {
      LAS char* pb = lds + wso + boff + (s4 & 1) * 2048;
#pragma unroll
      for (int j = 0; j < 3; ++j) fb[j] = lds_frag_tr(pb + j * 4096, 512);
    } else {
      LAS char* pb = lds + wso + (boff ^ (s4 * 32));
      fb[0] = lds_frag(pb); fb[1] = lds_frag(pb + 64 * 128);
    }
  };
  auto mm = [&](bf16x8 (&fa)[2], bf16x8 (&fb)[NT]) {
#pragma unroll
    for (int m = 0; m < 2; ++m) {
      if constexpr (ISA) {
        acc[m][0] = LMFMA(fb[0], fa[m], acc[m][0]);
        acc[m][1] = LMFMA(fb[1], fa[m], acc[m][1]);
        acc[m][2] = LMFMA(fa[m], fb[2], acc[m][2]);
      } else {
        acc[m][0] = LMFMA(fa[m], fb[0], acc[m][0]);
        acc[m][1] = LMFMA(fa[m], fb[1], acc[m][1]);
      }
    }
  };
  auto waitv = [&](auto a_, auto b_) {
    constexpr int a = decltype(a_)::value, b = decltype(b_)::value;
    if constexpr (ISA) { if (w < 4) wait_vm<2 * a + b>(); else wait_vm<a + b>(); }
    else wait_vm<b>();
  };
#define IC(n) std::integral_constant<int, (n)>{}
  auto wnext = [&](int o) { return o + WSLOT == NW * WSLOT ? 0 : o + WSLOT; };
  auto xnext = [&](int o) { return o == 65536 ? 0 : o + 32768; };
  if constexpr (ISA) {
    issue_w(0, 0); issue_x(0, 0); issue_w(1, WSLOT); issue_w(2, 2 * WSLOT); issue_x(1, 32768);
    issue_w(3, 3 * WSLOT); issue_w(4, 4 * WSLOT); issue_x(2, 65536);
    waitv(IC(4), IC(8));
  } else {
    issue_w(0, 0); issue_x(0, 0); issue_w(1, WSLOT); issue_x(1, 32768); issue_w(2, 2 * WSLOT); issue_x(2, 65536);
    waitv(IC(0), IC(12));
  }
  bar();
  rd(fa0, fb0, 0, 0, 0);
  wait_lgkm0();
  int xso = 0, wso = 0;
  auto tile = [&](int T, auto ix_, auto iw0_, auto iw1_, auto a0_, auto b0_, auto a1_, auto b1_, auto last_) {
    constexpr bool IX = decltype(ix_)::value, IW0 = decltype(iw0_)::value, IW1 = decltype(iw1_)::value, LAST = decltype(last_)::value;
    const int xsn = xnext(xso);
    if constexpr (ISA) {
      const int ws1 = wnext(wso), ws2 = wnext(ws1);
      rd(fa1, fb1, xso, wso, 1); mm(fa0, fb0); interleave<NM, 0, ND>(); SB();
      wait_lgkm0(); waitv(a0_, b0_); bar();
      if constexpr (IW0) issue_w(2 * T + 5, wso);
      rd(fa0, fb0, xso, ws1, 2); mm(fa1, fb1); interleave<NM, IW0 ? 2 : 0, ND>(); SB();
      wait_lgkm0();
      rd(fa1, fb1, xso, ws1, 3); mm(fa0, fb0); interleave<NM, 0, ND>(); SB();
      wait_lgkm0();
      if constexpr (!LAST) {
        waitv(a1_, b1_); bar();
        if constexpr (IW1) issue_w(2 * T + 6, ws1);
        if constexpr (IX) issue_x(T + 3, xso);
        rd(fa0, fb0, xsn, ws2, 0);
      }
      mm(fa1, fb1); interleave<NM, (IW1 ? 2 : 0) + (IX ? 4 : 0), LAST ? 0 : ND>(); SB();
      wait_lgkm0();
      wso = ws2;
    } else {
      const int ws1 = wnext(wso);
      rd(fa1, fb1, xso, wso, 1); mm(fa0, fb0); interleave<NM, 0, ND>(); SB();
      wait_lgkm0();
      rd(fa0, fb0, xso, wso, 2); mm(fa1, fb1); interleave<NM, 0, ND>(); SB();
      wait_lgkm0();
      rd(fa1, fb1, xso, wso, 3); mm(fa0, fb0); interleave<NM, 0, ND>(); SB();
      wait_lgkm0();
      if constexpr (!LAST) {
        waitv(a1_, b1_); bar();
        if constexpr (IX) { issue_w(T + 3, wso); issue_x(T + 3, xso); }
        rd(fa0, fb0, xsn, ws1, 0);
      }
      mm(fa1, fb1); interleave<NM, IX ? 6 : 0, LAST ? 0 : ND>(); SB();
      wait_lgkm0();
      wso = ws1;
    }
    xso = xsn;
  };
  constexpr std::true_type Y{};
  constexpr std::false_type N{};
  constexpr int XB = ISA ? 4 : 6;
  for (int T = 0; T < 13; ++T) tile(T, Y, Y, Y, IC(3), IC(8), IC(3), IC(XB), N);
  tile(13, N, Y, N, IC(3), IC(8), IC(3), IC(XB), N);
  tile(14, N, N, N, IC(2), IC(4), IC(1), IC(0), N);
  tile(15, N, N, N, IC(0), IC(0), IC(0), IC(0), Y);
}

template <bool ISA>
DI void p2_gemm(LAS char* lds, const char* xg0, const char* xg1, const char* yg, f32x16 (&acc)[2][2], int w, int lane) {
  constexpr int NS = 5, STAGE = 32768, NK = ISA ? 16 : 32;
  const int kh = w >> 2, wx = (w >> 1) & 1, wy = w & 1, h = lane >> 5, rho = lane & 31;
  u32 xoff[2], yoff[2];
#pragma unroll
  for (int a = 0; a < 2; ++a) {
    const int ix = 2 * w + a;
    if constexpr (ISA) {
      xoff[a] = (u32)((ix >> 2) * (16 * 4096) + (ix & 3) * 1024 + lane * 16);
      const int k = 8 * (ix & 7) + (lane >> 3);
      yoff[a] = (u32)(k * 2048 + (w >> 2) * 128 + (((lane & 7) ^ (((k >> 1) & 1) << 2)) * 16));
    } else {
      const int r = 8 * ix + (lane >> 3);
      xoff[a] = (u32)(r * 2048 + (((lane & 7) ^ ((r >> 1) & 7)) * 16));
      yoff[a] = (u32)((w >> 2) * (64 * 4096) + (ix & 7) * 1024 + lane * 16);
    }
  }
  auto issue = [&](int st, int so) {
    LAS char* s = lds + so + w * 2048;
    const char* xs = ISA ? xg0 + st * 4096 : ((st & 1) ? xg1 : xg0) + (st >> 1) * 128;
    const char* ys = ISA ? yg + (size_t)st * (64 * 2048) : yg + st * 8192;
    dma16(xs + xoff[0], s); dma16(xs + xoff[1], s + 1024);
    dma16(ys + yoff[0], s + 16384); dma16(ys + yoff[1], s + 16384 + 1024);
  };
  const int blk = (lane >> 4) & 1, q = (lane & 15) >> 2, p = lane & 3;
  int xo, yo;
  if constexpr (ISA) {
    xo = wx * 8192 + kh * 2048 + h * 512 + rho * 16;
    yo = 16384 + wy * 8192 + (32 * kh + 8 * h + q) * 128 + (((2 * blk + (p >> 1)) ^ (((q >> 1) & 1) << 2)) * 16) + 8 * (p & 1);
  } else {
    const int xrow = wx * 64 + rho;
    xo = xrow * 128 + (((4 * kh + h) ^ ((xrow >> 1) & 7)) * 16);
    yo = 16384 + wy * 8192 + kh * 4096 + (2 * h * 8 + 2 * blk + (p >> 1)) * 64 + q * 16 + 8 * (p & 1);
  }
  bf16x8 fx0[2], fy0[2], fx1[2], fy1[2];
  auto rd = [&](bf16x8 (&fx)[2], bf16x8 (&fy)[2], int so, int step) {
    LAS char* s = lds + so;
    if constexpr (ISA) {
      LAS char* px = s + xo + step * 1024;
      fx[0] = lds_frag(px); fx[1] = lds_frag(px + 4096);
      fy[0] = lds_frag_tr(s + yo + step * 2048, 512);
      fy[1] = lds_frag_tr(s + (yo ^ 64) + step * 2048, 512);
    } else {
      LAS char* px = s + (xo ^ (step * 32));
      fx[0] = lds_frag(px); fx[1] = lds_frag(px + 32 * 128);
      fy[0] = lds_frag_tr(s + yo + step * 2048, 512);
      fy[1] = lds_frag_tr(s + yo + step * 2048 + 256, 512);
    }
  };
  auto mm = [&](bf16x8 (&fx)[2], bf16x8 (&fy)[2]) {
#pragma unroll
    for (int i = 0; i < 2; ++i)
#pragma unroll
      for (int j = 0; j < 2; ++j)
        acc[i][j] = ISA ? LMFMA(fx[i], fy[j], acc[i][j]) : LMFMA(fy[j], fx[i], acc[i][j]);
  };
#pragma unroll
  for (int s = 0; s < NS; ++s) issue(s, s * STAGE);
  wait_vm<4 * (NS - 1)>();
  bar();
  rd(fx0, fy0, 0, 0);
  wait_lgkm0();
  int so = 0;
  auto stage = [&](int t, auto iss_, auto n_, auto last_) {
    constexpr bool ISS = decltype(iss_)::value, LAST = decltype(last_)::value;
    const int sn = so == (NS - 1) * STAGE ? 0 : so + STAGE;
    rd(fx1, fy1, so, 1); mm(fx0, fy0); interleave<4, 0, 6>(); SB();
    wait_lgkm0();
    if constexpr (!LAST) {
      wait_vm<4 * decltype(n_)::value>(); bar();
      if constexpr (ISS) issue(t + NS, so);
      rd(fx0, fy0, sn, 0);
    }
    mm(fx1, fy1); interleave<4, ISS ? 4 : 0, LAST ? 0 : 6>(); SB();
    wait_lgkm0();
    so = sn;
  };
  constexpr std::true_type Y{};
  constexpr std::false_type N{};
  for (int t = 0; t < NK - NS; ++t) stage(t, Y, IC(NS - 2), N);
  stage(NK - 5, N, IC(3), N); stage(NK - 4, N, IC(2), N); stage(NK - 3, N, IC(1), N); stage(NK - 2, N, IC(0), N);
  stage(NK - 1, N, IC(0), Y);
}

DI void p2_combine(LAS char* lds, f32x16 (&acc)[2][2], int w, int lane) {
  const int kh = w >> 2;
  bar();
  LAS char* mine = lds + w * 8192 + lane * 16;
  LAS char* theirs = lds + (w ^ 4) * 8192 + lane * 16;
#pragma unroll
  for (int j = 0; j < 2; ++j)
#pragma unroll
    for (int g = 0; g < 4; ++g) {
      const f32x16& src = kh ? acc[0][j] : acc[1][j];
      f32x4 v = {src[4 * g], src[4 * g + 1], src[4 * g + 2], src[4 * g + 3]};
      *(LAS f32x4*)(mine + (j * 4 + g) * 1024) = v;
    }
  wait_lgkm0();
  bar();
#pragma unroll
  for (int j = 0; j < 2; ++j) {
    f32x16 keep = kh ? acc[1][j] : acc[0][j];
#pragma unroll
    for (int g = 0; g < 4; ++g) {
      f32x4 v = *(LAS f32x4*)(theirs + (j * 4 + g) * 1024);
      keep[4 * g] += v[0]; keep[4 * g + 1] += v[1]; keep[4 * g + 2] += v[2]; keep[4 * g + 3] += v[3];
    }
    acc[0][j] = keep;
  }
}

#define STAMP(i)

DI void zero16(f32x16& v) {
#pragma unroll
  for (int i = 0; i < 16; ++i) v[i] = 0.f;
}

template <int PHASES>
__global__ __launch_bounds__(512, 2) void fused_kernel(Params P) {
  extern __shared__ __attribute__((aligned(16))) char smem_raw[];
  LAS char* lds = (LAS char*)smem_raw;
  const int tid = threadIdx.x, lane = tid & 63;
  const int w = __builtin_amdgcn_readfirstlane(tid >> 6);
  const int h = lane >> 5, rho = lane & 31;
  const int bid = blockIdx.x, xcd = bid & 7, idx = bid >> 3;
  const bool isA = idx < 16;
  const int li = idx & 15;
  const int c = 4 * (xcd & 1) + (li & 3), cg = 4 * (xcd >> 1) + (li >> 2);
  const int rb = 4 * (xcd & 3) + (li & 3), nb = 4 * (xcd >> 2) + (li >> 2);
  char* ws = P.ws;
  STAMP(0);
  const __amdgpu_buffer_rsrc_t wsr = __builtin_amdgcn_make_buffer_rsrc(ws, 0, (int)WS_END, 0x00020000);
  GAS u64* epoch = (GAS u64*)(ws + (isA ? WS_EPOCH_A : WS_EPOCH_B));
  u64 epoch_val = __hip_atomic_load(epoch, RLX_AGENT);

  if (isA) {
    if constexpr (PHASES & 1) {
      const int wm = w >> 1, hh = w & 1, hp = cg, head = 2 * hp + hh;
      u16 wraw[16];
#pragma unroll
      for (int j = 0; j < 16; ++j) wraw[j] = P.wphi[(head * 32 + 16 * (j >> 3) + 8 * ((j >> 2) & 1) + 4 * h + (j & 3)) * 32 + rho];
      f32x16 acc[2][3];
#pragma unroll
      for (int m = 0; m < 2; ++m)
#pragma unroll
        for (int j = 0; j < 3; ++j) zero16(acc[m][j]);
      p1_gemm<true>(lds, (const char*)(P.Ax + (size_t)c * 256 * 1024), (const char*)(P.wqkv + hp * 64), nullptr, acc, w, lane);
      wait_lgkm0();
      bar();
      STAMP(2);
      asm volatile("" : "+v"(epoch_val));
      const u64 tag = next_tag(epoch_val);
      bf16x8 wph[2];
#pragma unroll
      for (int j = 0; j < 16; ++j) asm volatile("" : "+v"(wraw[j]));
#pragma unroll
      for (int t = 0; t < 2; ++t) {
        u32x4 v;
#pragma unroll
        for (int j = 0; j < 4; ++j) v[j] = (u32)wraw[8 * t + 2 * j] | ((u32)wraw[8 * t + 2 * j + 1] << 16);
        wph[t] = __builtin_bit_cast(bf16x8, v);
      }
      bf16x8 phiq[2][4];
      f32x16 kv[2];
      zero16(kv[0]); zero16(kv[1]);
      float z[2] = {0.f, 0.f};
      LAS char* img = lds + 69632 + w * 4352;
      const int blk = (lane >> 4) & 1, q = (lane & 15) >> 2, p = lane & 3;
      const u32 ones = 0x3f803f80u;
#pragma unroll
      for (int m = 0; m < 2; ++m) {
        f32x16 pp, ep, em;
        zero16(pp);
        pp = MFMA(wph[0], pack8(acc[m][0], 0), pp);
        pp = MFMA(wph[1], pack8(acc[m][0], 1), pp);
        dual_softmax(pp, ep, em);
        phiq[m][0] = pack8(ep, 0); phiq[m][1] = pack8(ep, 1); phiq[m][2] = pack8(em, 0); phiq[m][3] = pack8(em, 1);
        zero16(pp);
        pp = MFMA(wph[0], pack8(acc[m][1], 0), pp);
        pp = MFMA(wph[1], pack8(acc[m][1], 1), pp);
        dual_softmax(pp, ep, em);
#pragma unroll
        for (int g = 0; g < 4; ++g) {
          u32x2 a = {pk2(ep[4 * g], ep[4 * g + 1]), pk2(ep[4 * g + 2], ep[4 * g + 3])};
          u32x2 b = {pk2(em[4 * g], em[4 * g + 1]), pk2(em[4 * g + 2], em[4 * g + 3])};
          *(LAS u32x2*)(img + rho * 136 + (8 * g + 4 * h) * 2) = a;
          *(LAS u32x2*)(img + rho * 136 + (32 + 8 * g + 4 * h) * 2) = b;
        }
#pragma unroll
        for (int t = 0; t < 2; ++t) {
          const bf16x8 vf = pack8(acc[m][2], t);
#pragma unroll
          for (int F = 0; F < 2; ++F) {
            const bf16x8 af = lds_frag_tr(img + (16 * t + 4 * h + q) * 136 + (32 * F + 16 * blk + 4 * p) * 2, 8 * 136);
            kv[F] = MFMA(af, vf, kv[F]);
            const u32x4 au = __builtin_bit_cast(u32x4, af);
#pragma unroll
            for (int e = 0; e < 4; ++e) asm("v_dot2c_f32_bf16 %0, %1, %2" : "+v"(z[F]) : "v"(au[e]), "v"(ones));
          }
        }
      }
      {
        LAS char* red = lds + w * 8448;
#pragma unroll
        for (int F = 0; F < 2; ++F) {
#pragma unroll
          for (int g = 0; g < 4; ++g) {
            f32x4 v = {kv[F][4 * g], kv[F][4 * g + 1], kv[F][4 * g + 2], kv[F][4 * g + 3]};
            *(LAS f32x4*)(red + ((F * 4 + g) * 64 + lane) * 16) = v;
          }
          const float zt = xhalf_sum(z[F]);
          if (h == 0) *(LAS float*)(red + 8192 + (F * 32 + rho) * 4) = zt;
        }
      }
      wait_lgkm0();
      bar();
      const u32 kvp_me = (u32)(WS_KVP + (size_t)(hp * 8 + c) * KVP_BYTES);
      {
#pragma unroll
        for (int e = 0; e < 2; ++e) {
          f32x4 s = *(LAS f32x4*)(lds + e * 8448 + tid * 16);
#pragma unroll
          for (int k = 1; k < 4; ++k) s += *(LAS f32x4*)(lds + (2 * k + e) * 8448 + tid * 16);
          __builtin_amdgcn_raw_buffer_store_b128(__builtin_bit_cast(u32x4, s), wsr, kvp_me + e * 8448 + tid * 16, 0, 16);
        }
        if (tid < 32) {
          const int e = tid >> 4, u = 512 + (tid & 15);
          f32x4 s = *(LAS f32x4*)(lds + e * 8448 + u * 16);
#pragma unroll
          for (int k = 1; k < 4; ++k) s += *(LAS f32x4*)(lds + (2 * k + e) * 8448 + u * 16);
          __builtin_amdgcn_raw_buffer_store_b128(__builtin_bit_cast(u32x4, s), wsr, kvp_me + e * 8448 + u * 16, 0, 16);
        }
      }
      wait_vm<0>();
      bar();
      if (tid == 0) __hip_atomic_store((GAS u64*)(ws + WS_KVFLAG + hp * 128 + c * 8), tag, RLX_AGENT);
      STAMP(3);
      if (w == 0) wait_flags(ws + WS_KVFLAG + hp * 128, 8, tag, lane);
      bar();
      STAMP(4);
      LAS char* kvb = lds + 73728;
      LAS char* zb = lds + 73728 + 8192;
      {
        const u32 kvp0 = (u32)(WS_KVP + (size_t)(hp * 8) * KVP_BYTES);
#pragma unroll
        for (int e = 0; e < 2; ++e) {
          f32x4 v[8];
#pragma unroll
          for (int k = 0; k < 8; ++k)
            v[k] = __builtin_bit_cast(f32x4, __builtin_amdgcn_raw_buffer_load_b128(wsr, kvp0 + k * (u32)KVP_BYTES + e * 8448 + tid * 16, 0, 16));
          f32x4 s = v[0];
#pragma unroll
          for (int k = 1; k < 8; ++k) s += v[k];
          u32x2 o = {pk2(s[0], s[1]), pk2(s[2], s[3])};
          *(LAS u32x2*)(kvb + e * 4096 + tid * 8) = o;
        }
        if (tid < 32) {
          const int e = tid >> 4, u = 512 + (tid & 15);
          f32x4 s = {0.f, 0.f, 0.f, 0.f};
#pragma unroll
          for (int k = 0; k < 8; ++k)
            s += __builtin_bit_cast(f32x4, __builtin_amdgcn_raw_buffer_load_b128(wsr, kvp0 + k * (u32)KVP_BYTES + e * 8448 + u * 16, 0, 16));
          *(LAS f32x4*)(zb + e * 256 + (tid & 15) * 16) = s;
        }
      }
      wait_lgkm0();
      bar();
      STAMP(9);
      bf16x8 kvf[4];
      f32x4 zz[2][4];
#pragma unroll
      for (int F = 0; F < 2; ++F) {
#pragma unroll
        for (int t = 0; t < 2; ++t) {
          u32x2 a = *(LAS u32x2*)(kvb + hh * 4096 + ((F * 4 + 2 * t) * 64 + lane) * 8);
          u32x2 b = *(LAS u32x2*)(kvb + hh * 4096 + ((F * 4 + 2 * t + 1) * 64 + lane) * 8);
          u32x4 v = {a[0], a[1], b[0], b[1]};
          kvf[2 * F + t] = __builtin_bit_cast(bf16x8, v);
        }
#pragma unroll
        for (int g = 0; g < 4; ++g) zz[F][g] = *(LAS f32x4*)(zb + hh * 256 + (F * 32 + 8 * g + 4 * h) * 4);
      }
#pragma unroll
      for (int m = 0; m < 2; ++m) {
        f32x16 ot;
        zero16(ot);
        float den = 0.f;
#pragma unroll
        for (int i = 0; i < 4; ++i) {
          ot = MFMA(kvf[i], phiq[m][i], ot);
          const u32x4 pu = __builtin_bit_cast(u32x4, phiq[m][i]);
#pragma unroll
          for (int e = 0; e < 4; ++e) {
            const f32x4 zv = zz[i >> 1][2 * (i & 1) + (e >> 1)];
            den = fmaf(__uint_as_float(pu[e] << 16), zv[2 * (e & 1)], den);
            den = fmaf(__uint_as_float(pu[e] & 0xffff0000u), zv[2 * (e & 1) + 1], den);
          }
        }
        den = xhalf_sum(den) + 1e-6f;
        const float r = 1.0f / den;
        u32 d[8];
#pragma unroll
        for (int i = 0; i < 8; ++i) d[i] = pk2(ot[2 * i] * r, ot[2 * i + 1] * r);
        const u32 obase = (u32)(WS_O + (size_t)((c * 8 + wm * 2 + m) * 16 + hp) * 4096);
#pragma unroll
        for (int i = 0; i < 2; ++i) {
          auto r0 = __builtin_amdgcn_permlane32_swap(d[4 * i], d[4 * i + 2], false, false);
          auto r1 = __builtin_amdgcn_permlane32_swap(d[4 * i + 1], d[4 * i + 3], false, false);
          u32x4 v = {r0[0], r1[0], r0[1], r1[1]};
          __builtin_amdgcn_raw_buffer_store_b128(v, wsr, obase + ((4 * hh + 2 * i + h) * 32 + rho) * 16, 0, 16);
        }
      }
      wait_vm<0>();
      bar();
      if (tid == 0) __hip_atomic_store((GAS u64*)(ws + WS_OFLAG + c * 128 + hp * 8), tag, RLX_AGENT);
      STAMP(5);
    }
    if constexpr (PHASES & 2) {
      const u64 tag = next_tag(epoch_val);
      if (w == 0) {
        wait_flags(ws + WS_OFLAG + (rb >> 1) * 128, 16, tag, lane);
        STAMP(10);
        __builtin_amdgcn_fence(__ATOMIC_ACQUIRE, "agent");
        wait_vm<0>();
        if (rb == 0 && nb == 0 && lane == 0) __hip_atomic_store(epoch, tag, RLX_AGENT);
      }
      bar();
      STAMP(6);
      f32x16 acc[2][2];
      zero16(acc[0][0]); zero16(acc[0][1]); zero16(acc[1][0]); zero16(acc[1][1]);
      p2_gemm<true>(lds, ws + WS_O + (size_t)rb * 4 * 16 * 4096, nullptr, (const char*)(P.wo + nb * 128), acc, w, lane);
      STAMP(7);
      p2_combine(lds, acc, w, lane);
      const int kh = w >> 2, wx = (w >> 1) & 1, wy = w & 1;
      float* op = P.out + (size_t)(rb * 128 + wx * 64 + kh * 32 + 4 * h) * 2048 + nb * 128 + wy * 64 + rho;
#pragma unroll
      for (int r = 0; r < 16; ++r)
#pragma unroll
        for (int j = 0; j < 2; ++j) op[((r & 3) + 8 * (r >> 2)) * 2048 + 32 * j] = acc[0][j][r];
      STAMP(8);
    }
  } else {
    if constexpr (PHASES & 1) {
      const int wm = w >> 1, pn = w & 1, pg = cg, pl = pn * 32 + rho, pidx = pg * 64 + pl;
      u16 raw_a = P.lre[pidx], raw_b = P.lim[pidx], raw_t = P.logdt[pidx];
      f32x16 acc[2][2];
      zero16(acc[0][0]); zero16(acc[0][1]); zero16(acc[1][0]); zero16(acc[1][1]);
      p1_gemm<false>(lds, (const char*)(P.Bx + (size_t)c * 256 * 1024), (const char*)(P.bre + (size_t)pg * 64 * 1024),
                     (const char*)(P.bim + (size_t)pg * 64 * 1024), acc, w, lane);
      STAMP(2);
      asm volatile("" : "+v"(epoch_val), "+v"(raw_a), "+v"(raw_b), "+v"(raw_t));
      const u64 tag = next_tag(epoch_val);
      const float la = bf2f(raw_a), lb = bf2f(raw_b), ldt = bf2f(raw_t);
      const float dt = bf2f((u16)(pk2(expf(ldt), 0.f) & 0xffffu));
      const float er = expf(dt * la);
      float sn, cs;
      sincosf(dt * lb, &sn, &cs);
      const float lr = er * cs, lim_ = er * sn;
      const float nr = lr - 1.0f, dn = 1.0f / (la * la + lb * lb);
      const float c2r = 2.0f * (nr * la + lim_ * lb) * dn, c2i = 2.0f * (lim_ * la - nr * lb) * dn;
      float pr = lr, pi = lim_;
#pragma unroll
      for (int i = 0; i < 5; ++i) { const float a = pr * pr - pi * pi, b = 2.0f * pr * pi; pr = a; pi = b; }
      float gr = 0.f, gi = 0.f;
#pragma unroll
      for (int m = 0; m < 2; ++m)
#pragma unroll
        for (int r = 0; r < 16; ++r) {
          const float a = fmaf(lr, gr, fmaf(-lim_, gi, acc[m][0][r]));
          const float b = fmaf(lr, gi, fmaf(lim_, gr, acc[m][1][r]));
          gr = a; gi = b;
        }
      wait_lgkm0();
      bar();
      const int sg = 2 * wm + h;
      LAS f32x2* E = (LAS f32x2*)lds;
      LAS f32x2* HIN = (LAS f32x2*)(lds + 4096);
      E[sg * 64 + pl] = (f32x2){gr, gi};
      wait_lgkm0();
      bar();
      float qr = 0.f, qi = 0.f, wr_ = 1.f, wi_ = 0.f;
#pragma unroll
      for (int k = 0; k < 7; ++k) {
        const f32x2 e = E[k * 64 + pl];
        const float a = fmaf(pr, qr, fmaf(-pi, qi, e[0])), b = fmaf(pr, qi, fmaf(pi, qr, e[1]));
        const float a2 = wr_ * pr - wi_ * pi, b2 = wr_ * pi + wi_ * pr;
        if (k < sg) { qr = a; qi = b; wr_ = a2; wi_ = b2; }
      }
      STAMP(3);
      if (wm == 3) {
        const u32 tg = (u32)tag;
        GAS u64* agg = (GAS u64*)(ws + WS_AGG) + (size_t)pg * 8 * 128;
        if (h == 1) {
          const float ar = fmaf(pr, qr, fmaf(-pi, qi, gr)), ai = fmaf(pr, qi, fmaf(pi, qr, gi));
          __hip_atomic_store(agg + c * 128 + pl, ((u64)tg << 32) | __float_as_uint(ar), RLX_AGENT);
          __hip_atomic_store(agg + c * 128 + 64 + pl, ((u64)tg << 32) | __float_as_uint(ai), RLX_AGENT);
        }
        float val[7];
        const u64 t0 = __builtin_amdgcn_s_memrealtime();
        for (;;) {
          bool ok = true;
#pragma unroll
          for (int k = 0; k < 7; ++k) {
            const u64 x = __hip_atomic_load(agg + (k < c ? k : c) * 128 + h * 64 + pl, RLX_AGENT);
            val[k] = __uint_as_float((u32)x);
            ok = ok && (k >= c || (u32)(x >> 32) == tg);
          }
          if (__all(ok)) break;
          if (__builtin_amdgcn_s_memrealtime() - t0 > 500000ull) break;
          __builtin_amdgcn_s_sleep(1);
        }
        float br = pr, bi = pi;
#pragma unroll
        for (int i = 0; i < 3; ++i) { const float a = br * br - bi * bi, b = 2.0f * br * bi; br = a; bi = b; }
        float hr = 0.f, hi = 0.f;
#pragma unroll
        for (int k = 0; k < 7; ++k) {
          auto r = __builtin_amdgcn_permlane32_swap(__float_as_uint(val[k]), __float_as_uint(val[k]), false, false);
          const float er_ = __uint_as_float(r[0]), ei_ = __uint_as_float(r[1]);
          const float a = fmaf(br, hr, fmaf(-bi, hi, er_)), b = fmaf(br, hi, fmaf(bi, hr, ei_));
          if (k < c) { hr = a; hi = b; }
        }
        if (h == 0) HIN[pl] = (f32x2){hr, hi};
      }
      wait_lgkm0();
      bar();
      STAMP(4);
      {
        const f32x2 hin = HIN[pl];
        gr = fmaf(wr_, hin[0], fmaf(-wi_, hin[1], qr));
        gi = fmaf(wr_, hin[1], fmaf(wi_, hin[0], qi));
      }
      const u32 hbase = (u32)(WS_H + (size_t)((((c * 4 + wm) * 16 + pg) * 2) * 2 + pn) * 4096) + (u32)(((rho >> 2) * 8 + 4 * h) * 64 + (rho & 3) * 16);
#pragma unroll
      for (int m = 0; m < 2; ++m)
#pragma unroll
        for (int half = 0; half < 2; ++half) {
          float hr[8], hi[8];
#pragma unroll
          for (int r = 0; r < 8; ++r) {
            const float a = fmaf(lr, gr, fmaf(-lim_, gi, acc[m][0][8 * half + r]));
            const float b = fmaf(lr, gi, fmaf(lim_, gr, acc[m][1][8 * half + r]));
            gr = a; gi = b;
            hr[r] = c2r * a - c2i * b;
            hi[r] = -(c2r * b + c2i * a);
          }
          u32x4 vr = {pk2(hr[0], hr[1]), pk2(hr[2], hr[3]), pk2(hr[4], hr[5]), pk2(hr[6], hr[7])};
          u32x4 vi = {pk2(hi[0], hi[1]), pk2(hi[2], hi[3]), pk2(hi[4], hi[5]), pk2(hi[6], hi[7])};
          const u32 o = hbase + (2 * m + half) * 64;
          __builtin_amdgcn_raw_buffer_store_b128(vr, wsr, o, 0, 16);
          __builtin_amdgcn_raw_buffer_store_b128(vi, wsr, o + 8192, 0, 16);
        }
      wait_vm<0>();
      bar();
      if (tid == 0) __hip_atomic_store((GAS u64*)(ws + WS_HFLAG + c * 128 + pg * 8), tag, RLX_AGENT);
      STAMP(5);
    }
    if constexpr (PHASES & 2) {
      const u64 tag = next_tag(epoch_val);
      if (w == 0) {
        wait_flags(ws + WS_HFLAG + (rb >> 1) * 128, 16, tag, lane);
        STAMP(10);
        __builtin_amdgcn_fence(__ATOMIC_ACQUIRE, "agent");
        wait_vm<0>();
        if (rb == 15 && nb == 0 && lane == 0) __hip_atomic_store(epoch, tag, RLX_AGENT);
      }
      bar();
      STAMP(6);
      f32x16 acc[2][2];
      zero16(acc[0][0]); zero16(acc[0][1]); zero16(acc[1][0]); zero16(acc[1][1]);
      p2_gemm<false>(lds, (const char*)(P.cre + (size_t)nb * 128 * 1024), (const char*)(P.cim + (size_t)nb * 128 * 1024),
                     ws + WS_H + (size_t)rb * 2 * 64 * 4096, acc, w, lane);
      STAMP(7);
      const int kh = w >> 2, wx = (w >> 1) & 1, wy = w & 1;
      const int d = nb * 128 + wx * 64 + kh * 32 + rho, t0 = rb * 128 + wy * 64 + 4 * h;
      u16 xv[2][16];
      const u16* xp = P.Bx + (size_t)t0 * 1024 + d;
#pragma unroll
      for (int j = 0; j < 2; ++j)
#pragma unroll
        for (int r = 0; r < 16; ++r) xv[j][r] = xp[(32 * j + (r & 3) + 8 * (r >> 2)) * 1024];
      const float dsk = bf2f(P.dskip[d]);
      p2_combine(lds, acc, w, lane);
      float* op = P.out + (size_t)t0 * 2048 + 1024 + d;
#pragma unroll
      for (int j = 0; j < 2; ++j)
#pragma unroll
        for (int r = 0; r < 16; ++r) op[(32 * j + (r & 3) + 8 * (r >> 2)) * 2048] = fmaf(bf2f(xv[j][r]), dsk, acc[0][j][r]);
      STAMP(8);
    }
  }
}

#define SPLIT_LAUNCH 0

extern "C" void kernel_launch(void* const* d_in, const int* in_sizes, int n_in,
                              void* d_out, int out_size, void* d_ws, size_t ws_size,
                              hipStream_t stream) {
  Params P;
  P.Ax = (const u16*)d_in[0]; P.wqkv = (const u16*)d_in[1]; P.wphi = (const u16*)d_in[2]; P.wo = (const u16*)d_in[3];
  P.Bx = (const u16*)d_in[4]; P.lre = (const u16*)d_in[5]; P.lim = (const u16*)d_in[6];
  P.bre = (const u16*)d_in[7]; P.bim = (const u16*)d_in[8]; P.cre = (const u16*)d_in[9]; P.cim = (const u16*)d_in[10];
  P.logdt = (const u16*)d_in[11]; P.dskip = (const u16*)d_in[12];
  P.out = (float*)d_out; P.ws = (char*)d_ws;
  (void)hipFuncSetAttribute((const void*)fused_kernel<3>, hipFuncAttributeMaxDynamicSharedMemorySize, LDS_BYTES);
  fused_kernel<3><<<256, 512, LDS_BYTES, stream>>>(P);
}
```

```cpp
#include <hip/hip_runtime.h>
#include <hip/hip_bf16.h>

typedef unsigned short u16;
typedef unsigned int u32;
typedef unsigned long long u64;
typedef __bf16 bf16x8 __attribute__((ext_vector_type(8)));
typedef __bf16 bf16x2 __attribute__((ext_vector_type(2)));
typedef short s16x4 __attribute__((ext_vector_type(4)));
typedef short s16x8 __attribute__((ext_vector_type(8)));
typedef float f32x16 __attribute__((ext_vector_type(16)));
typedef float f32x4 __attribute__((ext_vector_type(4)));
typedef float f32x2 __attribute__((ext_vector_type(2)));
typedef u32 u32x4 __attribute__((ext_vector_type(4)));
typedef u32 u32x2 __attribute__((ext_vector_type(2)));
#define LAS __attribute__((address_space(3)))
#define GAS __attribute__((address_space(1)))
#define DI __device__ __forceinline__
#define RLX_AGENT __ATOMIC_RELAXED, __HIP_MEMORY_SCOPE_AGENT

constexpr size_t WS_EPOCH_A = 0;
constexpr size_t WS_EPOCH_B = 256;
constexpr size_t WS_KVFLAG = 4096;
constexpr size_t WS_OFLAG = WS_KVFLAG + 16 * 128;
constexpr size_t WS_HFLAG = WS_OFLAG + 8 * 128;
constexpr size_t WS_AGG = 65536;
constexpr size_t WS_KVP = WS_AGG + 16 * 8 * 1024;
constexpr size_t KVP_BYTES = 2 * (2048 + 64) * 4;
constexpr size_t WS_O = 4u << 20;
constexpr size_t WS_H = 8u << 20;
constexpr size_t WS_END = 16u << 20;

constexpr int LDS_BYTES = 163840;

struct Params {
  const u16* Ax; const u16* wqkv; const u16* wphi; const u16* wo;
  const u16* Bx; const u16* lre; const u16* lim; const u16* bre; const u16* bim;
  const u16* cre; const u16* cim; const u16* logdt; const u16* dskip;
  float* out; char* ws;
};

DI float bf2f(u16 v) { return __uint_as_float(((u32)v) << 16); }
DI u32 pk2(float a, float b) {
  f32x2 v = {a, b};
  bf16x2 r = __builtin_convertvector(v, bf16x2);
  return __builtin_bit_cast(u32, r);
}
DI bf16x8 pack8(const f32x16& x, int s) {
  u32x4 p;
  p[0] = pk2(x[8 * s + 0], x[8 * s + 1]); p[1] = pk2(x[8 * s + 2], x[8 * s + 3]);
  p[2] = pk2(x[8 * s + 4], x[8 * s + 5]); p[3] = pk2(x[8 * s + 6], x[8 * s + 7]);
  return __builtin_bit_cast(bf16x8, p);
}
#define MFMA(a, b, c) __builtin_amdgcn_mfma_f32_32x32x16_bf16((a), (b), (c), 0, 0, 0)
#define LMFMA MFMA
DI bf16x8 lds_frag(LAS char* p) { return *(LAS bf16x8*)p; }
DI s16x4 lds_tr(LAS char* __restrict__ p) { return __builtin_amdgcn_ds_read_tr16_b64_v4i16((LAS s16x4*)p); }
DI bf16x8 lds_frag_tr(LAS char* p, int off2) {
  s16x4 lo = lds_tr(p), hi = lds_tr(p + off2);
  s16x8 v = __builtin_shufflevector(lo, hi, 0, 1, 2, 3, 4, 5, 6, 7);
  return __builtin_bit_cast(bf16x8, v);
}
template <int OFF = 0> DI void dma_s(u32 voff, const char* sbase, u32 ldsaddr) {
  asm volatile("s_mov_b32 m0, %2\n\ts_nop 0\n\tglobal_load_lds_dwordx4 %0, %1 offset:%3" ::"v"(voff), "s"(sbase), "s"(ldsaddr), "n"(OFF) : "memory");
}
DI void dma_s4_dev(u32 voff, const char* sbase, u32 ldsaddr) {
  asm volatile("s_mov_b32 m0, %2\n\ts_nop 0\n\tglobal_load_lds_dwordx4 %0, %1 sc1\n\tglobal_load_lds_dwordx4 %0, %1 offset:1024 sc1\n\t"
               "global_load_lds_dwordx4 %0, %1 offset:2048 sc1\n\tglobal_load_lds_dwordx4 %0, %1 offset:3072 sc1"
               ::"v"(voff), "s"(sbase), "s"(ldsaddr) : "memory");
}
template <int N> DI void wait_vm() { asm volatile("s_waitcnt vmcnt(%0)" ::"n"(N) : "memory"); }
DI void wait_lgkm0() { asm volatile("" ::: "memory"); __builtin_amdgcn_s_waitcnt(0xC07F); asm volatile("" ::: "memory"); }
#define SB() __builtin_amdgcn_sched_barrier(0)
DI void bar() { asm volatile("" ::: "memory"); __builtin_amdgcn_s_barrier(); asm volatile("" ::: "memory"); }
DI float xhalf_max(float v) {
  auto r = __builtin_amdgcn_permlane32_swap(__float_as_uint(v), __float_as_uint(v), false, false);
  return fmaxf(__uint_as_float(r[0]), __uint_as_float(r[1]));
}
DI float xhalf_min(float v) {
  auto r = __builtin_amdgcn_permlane32_swap(__float_as_uint(v), __float_as_uint(v), false, false);
  return fminf(__uint_as_float(r[0]), __uint_as_float(r[1]));
}
DI float xhalf_sum(float v) {
  auto r = __builtin_amdgcn_permlane32_swap(__float_as_uint(v), __float_as_uint(v), false, false);
  return __uint_as_float(r[0]) + __uint_as_float(r[1]);
}
DI u64 next_tag(u64 e) { return e * 6364136223846793005ull + 1442695040888963407ull; }

DI void wait_flags(const char* flags, int n, u64 tag, int lane) {
  const GAS u64* f = (const GAS u64*)flags + (lane < n ? lane : 0);
  const u64 t0 = __builtin_amdgcn_s_memrealtime();
  for (;;) {
    u64 v = __hip_atomic_load(f, RLX_AGENT);
    if (__all(v == tag)) break;
    if (__builtin_amdgcn_s_memrealtime() - t0 > 500000ull) break;
    __builtin_amdgcn_s_sleep(1);
  }
}

DI void dual_softmax(const f32x16& p, f32x16& ep, f32x16& em) {
  float mx = p[0], mn = p[0];
#pragma unroll
  for (int i = 1; i < 16; ++i) { mx = fmaxf(mx, p[i]); mn = fminf(mn, p[i]); }
  mx = xhalf_max(mx); mn = xhalf_min(mn);
  const float L2E = 1.4426950408889634f;
  const float bp = -mx * L2E, bm = mn * L2E;
  float sp = 0.f, sm = 0.f;
#pragma unroll
  for (int i = 0; i < 16; ++i) {
    ep[i] = __builtin_amdgcn_exp2f(fmaf(p[i], L2E, bp));
    em[i] = __builtin_amdgcn_exp2f(fmaf(p[i], -L2E, bm));
    sp += ep[i]; sm += em[i];
  }
  sp = xhalf_sum(sp); sm = xhalf_sum(sm);
  const float rp = 1.0f / sp, rm = 1.0f / sm;
#pragma unroll
  for (int i = 0; i < 16; ++i) { ep[i] *= rp; em[i] *= rm; }
}

#define FRONT -1
template <int NM, int ND> DI void interleave() {
  constexpr int FR = FRONT > 0 ? FRONT : NM + FRONT;
  constexpr int DPM = (ND + FR - 1) / FR;
#pragma unroll
  for (int i = 0; i < NM; ++i) {
    __builtin_amdgcn_sched_group_barrier(0x008, 1, 0);
    if (i * DPM < ND) __builtin_amdgcn_sched_group_barrier(0x100, DPM, 0);
  }
}
#define IC(n) std::integral_constant<int, (n)>{}
constexpr int LDS_DUMP = 159744;
DI void prefetch_lines(u32 voff, const char* sbase, u32 dump) {
  asm volatile("s_mov_b32 m0, %2\n\ts_nop 0\n\tglobal_load_lds_dword %0, %1" ::"v"(voff), "s"(sbase), "s"(dump) : "memory");
}

constexpr int P1_WOFF = 98304;
#define PFT 9
#define PFIN 0
#define PFQ 3

template <int N, class F, int I = 0> DI void static_for(F&& f) {
  if constexpr (I < N) { f(std::integral_constant<int, I>{}); static_for<N, F, I + 1>(static_cast<F&&>(f)); }
}

template <bool ISA, class HOOK>
DI void p1_load(u32 ldsb, const char* xg, const char* wg0, const char* wg1, int lw, int lane, HOOK&& hook) {
  constexpr int WSLOT = ISA ? 12288 : 16384, NW = ISA ? 5 : 3;
  const u32 offE = (u32)((lane >> 3) * 2048 + (((lane & 7) ^ (lane >> 4)) * 16));
  const u32 offO = (u32)((8 + (lane >> 3)) * 2048 + (((lane & 7) ^ (4 + (lane >> 4))) * 16));
  const u32 offK = (u32)((lane >> 3) * 6144 + (((lane & 7) ^ (((lane >> 4) & 1) << 2)) * 16));
  const char* xb = xg + lw * (64 * 2048);
  const char* wb = ISA ? wg0 + lw * (8 * 6144) : (lw < 2 ? wg0 : wg1) + (lw & 1) * (32 * 2048);
  auto issue_x = [&](int T, int so) {
    const u32 l = ldsb + so + lw * 8192;
    const char* g = xb + T * 128;
#pragma unroll
    for (int a = 0; a < 4; ++a) { dma_s(offE, g + a * 32768, l + a * 2048); dma_s(offO, g + a * 32768, l + a * 2048 + 1024); }
  };
  auto issue_w = [&](int u, int so) {
    if constexpr (ISA) {
      const u32 l = ldsb + P1_WOFF + so + lw * 1024;
      const char* g = wb + (size_t)u * (32 * 6144);
      dma_s(offK, g, l); dma_s(offK, g + 2048, l + 4096); dma_s(offK, g + 4096, l + 8192);
    } else {
      const u32 l = ldsb + P1_WOFF + so + lw * 4096;
      const char* g = wb + u * 128;
      dma_s(offE, g, l); dma_s(offO, g, l + 1024); dma_s(offE, g + 32768, l + 2048); dma_s(offO, g + 32768, l + 3072);
    }
  };
  auto wnext = [&](int o) { return o + WSLOT == NW * WSLOT ? 0 : o + WSLOT; };
  auto xnext = [&](int o) { return o == 65536 ? 0 : o + 32768; };
  int xso = 0, wso = 0;
  if constexpr (ISA) {
    issue_w(0, 0); issue_x(0, 0); issue_w(1, WSLOT);
    wait_vm<3>(); bar();
    issue_w(2, 2 * WSLOT); issue_x(1, 32768); issue_w(3, 3 * WSLOT); issue_w(4, 4 * WSLOT); issue_x(2, 65536);
#pragma unroll 1
    for (int T = 0; T < 13; ++T) {
      const int ws1 = wnext(wso);
      wait_vm<9 + 16>(); bar();
      issue_w(2 * T + 5, wso);
      wait_vm<9 + 8>(); bar();
      issue_w(2 * T + 6, ws1); issue_x(T + 3, xso);
      wso = wnext(ws1); xso = xnext(xso);
    }
    wait_vm<9 + 16>(); bar();
    issue_w(31, wso);
    wait_vm<9 + 8>(); bar();
    hook(IC(0));
    wait_vm<6 + 8 + 1>(); bar();
    wait_vm<3 + 1>(); bar();
    wait_vm<0>(); bar();
  } else {
    issue_w(0, 0); issue_x(0, 0); issue_w(1, WSLOT); issue_x(1, 32768);
    wait_vm<12>(); bar();
    issue_w(2, 2 * WSLOT); issue_x(2, 65536);
#pragma unroll 1
    for (int T = 0; T < 13; ++T) {
      wait_vm<12>(); bar();
      issue_w(T + 3, wso); issue_x(T + 3, xso);
      wso = wnext(wso); xso = xnext(xso);
    }
    wait_vm<12>(); bar();
    wait_vm<0>(); bar();
  }
}

template <bool ISA, class ST, class PF>
DI void p1_mma(LAS char* lds, f32x16 (&acc)[2][ISA ? 3 : 2], int w, int lane, ST&& st, PF&& pf) {
  constexpr int NT = ISA ? 3 : 2;
  constexpr int WSLOT = ISA ? 12288 : 16384;
  constexpr int NM = 2 * NT, ND = ISA ? 8 : 4;
  const int wm = w >> 1, wn = w & 1, h = lane >> 5, rho = lane & 31;
  const int arow = wm * 64 + (ISA ? rho : (32 * ((rho >> 2) & 1) + (rho & 3) + 4 * (rho >> 3)));
  constexpr int AM = ISA ? 32 * 128 : 16 * 128;
  const int aoff = arow * 128 + ((h ^ ((arow >> 1) & 7)) * 16);
  int boff;
  if constexpr (ISA) {
    const int blk = (lane >> 4) & 1, q = (lane & 15) >> 2, p = lane & 3;
    const int c = (4 * wn + 2 * blk + (p >> 1)) ^ (((q >> 1) & 1) << 2);
    boff = P1_WOFF + (8 * h + q) * 128 + c * 16 + 8 * (p & 1);
  } else {
    const int brow = wn * 32 + rho;
    boff = P1_WOFF + brow * 128 + ((h ^ ((brow >> 1) & 7)) * 16);
  }
  LAS char* pa[2][4];
  LAS char* pb[4];
#pragma unroll
  for (int s4 = 0; s4 < 4; ++s4) {
    pa[0][s4] = lds + (aoff ^ (s4 * 32)); pa[1][s4] = pa[0][s4] + 65536;
    pb[s4] = lds + (ISA ? boff : (boff ^ (s4 * 32)));
    asm volatile("" : "+v"(pa[0][s4]), "+v"(pa[1][s4]));
    if (!ISA || s4 == 0) asm volatile("" : "+v"(pb[s4]));
  }
  bf16x8 fa0[2], fb0[NT], fa1[2], fb1[NT];
  auto rd = [&](bf16x8 (&fa)[2], bf16x8 (&fb)[NT], auto T_, auto s4_) {
    constexpr int T = decltype(T_)::value, s4 = decltype(s4_)::value;
    constexpr int xs = T % 3;
    LAS char* a = pa[xs >> 1][s4] + (xs & 1) * 32768;
    fa[0] = lds_frag(a); fa[1] = lds_frag(a + AM);
    if constexpr (ISA) {
      LAS char* b = pb[0] + ((2 * T + (s4 >> 1)) % 5) * WSLOT + (s4 & 1) * 2048;
#pragma unroll
      for (int j = 0; j < 3; ++j) fb[j] = lds_frag_tr(b + j * 4096, 512);
    } else {
      LAS char* b = pb[s4] + xs * WSLOT;
      fb[0] = lds_frag(b); fb[1] = lds_frag(b + 64 * 128);
    }
  };
  auto mm = [&](bf16x8 (&fa)[2], bf16x8 (&fb)[NT]) {
#pragma unroll
    for (int m = 0; m < 2; ++m) {
      if constexpr (ISA) {
        acc[m][0] = LMFMA(fb[0], fa[m], acc[m][0]);
        acc[m][1] = LMFMA(fb[1], fa[m], acc[m][1]);
        acc[m][2] = LMFMA(fa[m], fb[2], acc[m][2]);
      } else {
        acc[m][0] = LMFMA(fa[m], fb[0], acc[m][0]);
        acc[m][1] = LMFMA(fa[m], fb[1], acc[m][1]);
      }
    }
  };
  bar();
  st();
  rd(fa0, fb0, IC(0), IC(0));
  static_for<16>([&](auto T_) {
    constexpr int T = decltype(T_)::value;
    rd(fa1, fb1, T_, IC(1)); mm(fa0, fb0); interleave<NM, ND>(); SB();
    if constexpr (ISA) { wait_lgkm0(); bar(); }
    rd(fa0, fb0, T_, IC(2)); mm(fa1, fb1); interleave<NM, ND>(); SB();
    pf(T_);
    rd(fa1, fb1, T_, IC(3)); mm(fa0, fb0); interleave<NM, ND>(); SB();
    if constexpr (T < 15) { wait_lgkm0(); bar(); rd(fa0, fb0, IC(T + 1), IC(0)); }
    mm(fa1, fb1); interleave<NM, T < 15 ? ND : 0>(); SB();
  });
}

template <bool ISA>
DI void p2_load(u32 ldsb, const char* xg0, const char* xg1, const char* yg, int lw, int lane,
                const char* flags, u64 tag, GAS u64* epoch_to_bump) {
  constexpr int NS = 4, STAGE = 32768, NK = ISA ? 16 : 32;
  const u32 lin = (u32)(lane * 16);
  u32 swE, swO;
  const char *wb0, *wb1, *hb;
  if constexpr (ISA) {
    swE = (u32)((lane >> 3) * 2048 + (((lane & 7) ^ (((lane >> 4) & 1) << 2)) * 16));
    swO = swE + 8 * 2048;
    wb0 = wb1 = yg + (lw & 1) * (32 * 2048) + (lw >> 1) * 128;
    hb = xg0 + lw * (16 * 4096);
  } else {
    swE = (u32)((lane >> 3) * 2048 + (((lane & 7) ^ (lane >> 4)) * 16));
    swO = (u32)((8 + (lane >> 3)) * 2048 + (((lane & 7) ^ (4 + (lane >> 4))) * 16));
    wb0 = xg0 + lw * (32 * 2048); wb1 = xg1 + lw * (32 * 2048);
    hb = yg + (lw >> 1) * (64 * 4096) + (lw & 1) * 4096;
  }
  auto issue_w = [&](int st, int so) {
    const u32 l = ldsb + so + (ISA ? 16384 : 0) + lw * 4096;
    const char* g = ISA ? wb0 + (size_t)st * (64 * 2048) : ((st & 1) ? wb1 : wb0) + (st >> 1) * 128;
    dma_s(swE, g, l); dma_s(swO, g, l + 1024); dma_s(swE, g + 32768, l + 2048); dma_s(swO, g + 32768, l + 3072);
  };
  auto issue_h = [&](int st, int so) {
    dma_s4_dev(lin, hb + st * (ISA ? 4096 : 8192), ldsb + so + (ISA ? 0 : 16384) + lw * 4096);
  };
#pragma unroll
  for (int s = 0; s < NS; ++s) issue_w(s, s * STAGE);
  if (lw == 0) {
    wait_flags(flags, 16, tag, lane);
    if (epoch_to_bump != nullptr && lane == 0) __hip_atomic_store(epoch_to_bump, tag, RLX_AGENT);
  }
  bar();
#pragma unroll
  for (int s = 0; s < NS; ++s) issue_h(s, s * STAGE);
  wait_vm<12>(); bar();
  int so = 0;
  auto nxt = [&]() { so = so == (NS - 1) * STAGE ? 0 : so + STAGE; };
  wait_vm<8>(); bar(); issue_h(NS + 0, so); issue_w(NS + 0, so); nxt();
  wait_vm<12>(); bar(); issue_h(NS + 1, so); issue_w(NS + 1, so); nxt();
#pragma unroll 1
  for (int t = 2; t < NK - NS; ++t) { wait_vm<16>(); bar(); issue_h(t + NS, so); issue_w(t + NS, so); nxt(); }
  wait_vm<16>(); bar(); wait_vm<8>(); bar(); wait_vm<0>(); bar();
  bar(); bar();
}

template <bool ISA, class ST>
DI void p2_mma(LAS char* lds, f32x16 (&acc)[2][2], int w, int lane, ST&& st) {
  constexpr int NS = 4, STAGE = 32768, NK = ISA ? 16 : 32;
  const int kh = w >> 2, wx = (w >> 1) & 1, wy = w & 1, h = lane >> 5, rho = lane & 31;
  const int blk = (lane >> 4) & 1, q = (lane & 15) >> 2, p = lane & 3;
  int xo, yo;
  if constexpr (ISA) {
    xo = wx * 8192 + kh * 2048 + h * 512 + rho * 16;
    yo = 16384 + wy * 8192 + (32 * kh + 8 * h + q) * 128 + (((2 * blk + (p >> 1)) ^ (((q >> 1) & 1) << 2)) * 16) + 8 * (p & 1);
  } else {
    const int xrow = wx * 64 + rho;
    xo = xrow * 128 + (((4 * kh + h) ^ ((xrow >> 1) & 7)) * 16);
    yo = 16384 + wy * 8192 + kh * 4096 + (2 * h * 8 + 2 * blk + (p >> 1)) * 64 + q * 16 + 8 * (p & 1);
  }
  LAS char* px[2][2];
  LAS char* py[2][2];
#pragma unroll
  for (int wdw = 0; wdw < 2; ++wdw) {
    px[wdw][0] = lds + wdw * 65536 + xo; px[wdw][1] = lds + wdw * 65536 + (xo ^ 32);
    py[wdw][0] = lds + wdw * 65536 + yo; py[wdw][1] = lds + wdw * 65536 + (yo ^ 64);
    asm volatile("" : "+v"(px[wdw][0]), "+v"(py[wdw][0]));
    if (ISA) asm volatile("" : "+v"(py[wdw][1])); else asm volatile("" : "+v"(px[wdw][1]));
  }
  bf16x8 fx0[2], fy0[2], fx1[2], fy1[2];
  auto rd = [&](bf16x8 (&fx)[2], bf16x8 (&fy)[2], auto slot_, auto step_) {
    constexpr int slot = decltype(slot_)::value, step = decltype(step_)::value;
    constexpr int wdw = slot >> 1, so = (slot & 1) * STAGE;
    if constexpr (ISA) {
      LAS char* x = px[wdw][0] + so + step * 1024;
      fx[0] = lds_frag(x); fx[1] = lds_frag(x + 4096);
      fy[0] = lds_frag_tr(py[wdw][0] + so + step * 2048, 512);
      fy[1] = lds_frag_tr(py[wdw][1] + so + step * 2048, 512);
    } else {
      LAS char* x = px[wdw][step] + so;
      fx[0] = lds_frag(x); fx[1] = lds_frag(x + 32 * 128);
      fy[0] = lds_frag_tr(py[wdw][0] + so + step * 2048, 512);
      fy[1] = lds_frag_tr(py[wdw][0] + so + step * 2048 + 256, 512);
    }
  };
  auto mm = [&](bf16x8 (&fx)[2], bf16x8 (&fy)[2]) {
#pragma unroll
    for (int i = 0; i < 2; ++i)
#pragma unroll
      for (int j = 0; j < 2; ++j)
        acc[i][j] = ISA ? LMFMA(fx[i], fy[j], acc[i][j]) : LMFMA(fy[j], fx[i], acc[i][j]);
  };
  bar();
  bar();
  st();
  rd(fx0, fy0, IC(0), IC(0));
  auto stage = [&](auto slot_, auto last_) {
    constexpr int slot = decltype(slot_)::value;
    constexpr bool LAST = decltype(last_)::value;
    rd(fx1, fy1, slot_, IC(1)); mm(fx0, fy0); interleave<4, 6>(); SB();
    if constexpr (!LAST) { wait_lgkm0(); bar(); rd(fx0, fy0, IC((slot + 1) % NS), IC(0)); }
    mm(fx1, fy1); interleave<4, LAST ? 0 : 6>(); SB();
  };
#pragma unroll 1
  for (int t = 0; t < NK / NS - 1; ++t) {
    stage(IC(0), std::false_type{}); stage(IC(1), std::false_type{}); stage(IC(2), std::false_type{}); stage(IC(3), std::false_type{});
  }
  stage(IC(0), std::false_type{}); stage(IC(1), std::false_type{}); stage(IC(2), std::false_type{}); stage(IC(3), std::true_type{});
}

DI void p2_combine(LAS char* lds, f32x16 (&acc)[2][2], int w, int lane) {
  const int kh = w >> 2;
  bar();
  LAS char* mine = lds + w * 8192 + lane * 16;
  LAS char* theirs = lds + (w ^ 4) * 8192 + lane * 16;
#pragma unroll
  for (int j = 0; j < 2; ++j)
#pragma unroll
    for (int g = 0; g < 4; ++g) {
      const f32x16& src = kh ? acc[0][j] : acc[1][j];
      f32x4 v = {src[4 * g], src[4 * g + 1], src[4 * g + 2], src[4 * g + 3]};
      *(LAS f32x4*)(mine + (j * 4 + g) * 1024) = v;
    }
  wait_lgkm0();
  bar();
#pragma unroll
  for (int j = 0; j < 2; ++j) {
    f32x16 keep = kh ? acc[1][j] : acc[0][j];
#pragma unroll
    for (int g = 0; g < 4; ++g) {
      f32x4 v = *(LAS f32x4*)(theirs + (j * 4 + g) * 1024);
      keep[4 * g] += v[0]; keep[4 * g + 1] += v[1]; keep[4 * g + 2] += v[2]; keep[4 * g + 3] += v[3];
    }
    acc[0][j] = keep;
  }
}

#define STAMP(i)
#define STAMP0(i)

DI void zero16(f32x16& v) {
#pragma unroll
  for (int i = 0; i < 16; ++i) v[i] = 0.f;
}

constexpr int LDS_WPHI = P1_WOFF + 2 * 12288;
constexpr int LDS_CONST = 147456;
constexpr int LDS_E = LDS_CONST + 2048;
constexpr int LDS_HIN = LDS_E + 4096;

__global__ __launch_bounds__(768, 3) void fused_kernel(Params P) {
  extern __shared__ __attribute__((aligned(16))) char smem_raw[];
  LAS char* lds = (LAS char*)smem_raw;
  const int tid = threadIdx.x, lane = tid & 63;
  const int w = __builtin_amdgcn_readfirstlane(tid >> 6);
  const int h = lane >> 5, rho = lane & 31;
  const int bid = blockIdx.x, xcd = bid & 7, idx = bid >> 3;
  const bool isA = idx < 16;
  const int li = idx & 15;
  const int c = 4 * (xcd & 1) + (li & 3), cg = 4 * (xcd >> 1) + (li >> 2);
  const int rb = 4 * (xcd & 3) + (li & 3), nb = 4 * (xcd >> 2) + (li >> 2);
  char* ws = P.ws;
  STAMP0(0);
  const __amdgpu_buffer_rsrc_t wsr = __builtin_amdgcn_make_buffer_rsrc(ws, 0, (int)WS_END, 0x00020000);
  GAS u64* epoch = (GAS u64*)(ws + (isA ? WS_EPOCH_A : WS_EPOCH_B));

  if (w >= 8) {
    const int lw = w - 8;
    const u32 ldsb = (u32)(size_t)lds;
    __builtin_amdgcn_s_setprio(3);
    u64 epoch_val = __hip_atomic_load(epoch, RLX_AGENT);
    if (isA) {
      const int hp = cg;
      p1_load<true>(ldsb, (const char*)(P.Ax + (size_t)c * 256 * 1024), (const char*)(P.wqkv + hp * 64), nullptr, lw, lane,
                    [&](auto) { dma_s((u32)(lane * 16), (const char*)(P.wphi + hp * 2048) + lw * 1024, ldsb + LDS_WPHI + lw * 1024); });
      asm volatile("" : "+v"(epoch_val));
      const u64 tag = next_tag(epoch_val);
      bar(); bar(); bar();
      if (lw == 0) {
        if (lane == 0) __hip_atomic_store((GAS u64*)(ws + WS_KVFLAG + hp * 128 + c * 8), tag, RLX_AGENT);
        wait_flags(ws + WS_KVFLAG + hp * 128, 8, tag, lane);
      }
      bar(); bar(); bar();
      if (lw == 0 && lane == 0) __hip_atomic_store((GAS u64*)(ws + WS_OFLAG + c * 128 + hp * 8), tag, RLX_AGENT);
      p2_load<true>(ldsb, ws + WS_O + (size_t)rb * 4 * 16 * 4096, nullptr, (const char*)(P.wo + nb * 128), lw, lane,
                    ws + WS_OFLAG + (rb >> 1) * 128, tag, (rb == 0 && nb == 0 && lw == 0) ? epoch : nullptr);
    } else {
      const int pg = cg;
      p1_load<false>(ldsb, (const char*)(P.Bx + (size_t)c * 256 * 1024), (const char*)(P.bre + (size_t)pg * 64 * 1024),
                     (const char*)(P.bim + (size_t)pg * 64 * 1024), lw, lane, [](auto) {});
      asm volatile("" : "+v"(epoch_val));
      const u64 tag = next_tag(epoch_val);
      bar();
      if (lw == 0) {
        const f32x2 k1 = *(LAS f32x2*)(lds + LDS_CONST + lane * 32 + 16);
        const float pr = k1[0], pi = k1[1];
        const u32 tg = (u32)tag;
        GAS u64* agg = (GAS u64*)(ws + WS_AGG) + (size_t)pg * 8 * 128;
        LAS f32x2* E = (LAS f32x2*)(lds + LDS_E);
        float ar = 0.f, ai = 0.f;
#pragma unroll
        for (int k = 0; k < 8; ++k) {
          const f32x2 e = E[k * 64 + lane];
          const float a = fmaf(pr, ar, fmaf(-pi, ai, e[0])), b = fmaf(pr, ai, fmaf(pi, ar, e[1]));
          ar = a; ai = b;
        }
        __hip_atomic_store(agg + c * 128 + lane, ((u64)tg << 32) | __float_as_uint(ar), RLX_AGENT);
        __hip_atomic_store(agg + c * 128 + 64 + lane, ((u64)tg << 32) | __float_as_uint(ai), RLX_AGENT);
        float br = pr, bi = pi;
#pragma unroll
        for (int i = 0; i < 3; ++i) { const float a = br * br - bi * bi, b = 2.0f * br * bi; br = a; bi = b; }
        float vr[7], vi[7];
        const u64 t0 = __builtin_amdgcn_s_memrealtime();
        for (;;) {
          u64 x[7], y[7];
#pragma unroll
          for (int k = 0; k < 7; ++k) {
            const int kk = k < c ? k : c;
            x[k] = __hip_atomic_load(agg + kk * 128 + lane, RLX_AGENT);
            y[k] = __hip_atomic_load(agg + kk * 128 + 64 + lane, RLX_AGENT);
          }
          u32 bad = 0;
#pragma unroll
          for (int k = 0; k < 7; ++k) {
            vr[k] = __uint_as_float((u32)x[k]); vi[k] = __uint_as_float((u32)y[k]);
            bad |= k < c ? (((u32)(x[k] >> 32) ^ tg) | ((u32)(y[k] >> 32) ^ tg)) : 0u;
          }
          if (__all(bad == 0)) break;
          if (__builtin_amdgcn_s_memrealtime() - t0 > 500000ull) break;
          __builtin_amdgcn_s_sleep(1);
        }
        float hr = 0.f, hi = 0.f;
#pragma unroll
        for (int k = 0; k < 7; ++k) {
          const float a = fmaf(br, hr, fmaf(-bi, hi, vr[k])), b = fmaf(br, hi, fmaf(bi, hr, vi[k]));
          if (k < c) { hr = a; hi = b; }
        }
        *(LAS f32x2*)(lds + LDS_HIN + lane * 8) = (f32x2){hr, hi};
        wait_lgkm0();
      }
      bar(); bar();
      if (lw == 0 && lane == 0) __hip_atomic_store((GAS u64*)(ws + WS_HFLAG + c * 128 + pg * 8), tag, RLX_AGENT);
      p2_load<false>(ldsb, (const char*)(P.cre + (size_t)nb * 128 * 1024), (const char*)(P.cim + (size_t)nb * 128 * 1024),
                     ws + WS_H + (size_t)rb * 2 * 64 * 4096, lw, lane,
                     ws + WS_HFLAG + (rb >> 1) * 128, tag, (rb == 15 && nb == 0 && lw == 0) ? epoch : nullptr);
    }
    return;
  }

  if (isA) {
    const int wm = w >> 1, hh = w & 1, hp = cg;
    bf16x8 phiq[2][4];
    {
      f32x16 acc[2][3];
#pragma unroll
      for (int m = 0; m < 2; ++m)
#pragma unroll
        for (int j = 0; j < 3; ++j) zero16(acc[m][j]);
      const u32 pfo = (u32)(lane * 128);
      const char* pfb = (const char*)P.wo + (size_t)((xcd * 16 + li) * 2 + (w & 1)) * 8192;
      const int gb = xcd * 16 + li;
      const u32 pfq = w == 2 ? (u32)((lane >> 2) * 2048 + (lane & 3) * 128) : (u32)((w == 3 ? lane : 64 + (lane & 31)) * 128);
      const char* pf1 = w == 2 ? (const char*)P.Ax + (size_t)gb * (16 * 2048) : (const char*)P.wqkv + (size_t)gb * 12288;
      const int pfstep = w == 2 ? 512 : 256 * 6144;
      p1_mma<true>(lds, acc, w, lane, [&]() { STAMP0(1); },
                   [&](auto T_) {
                     constexpr int T = decltype(T_)::value;
                     if constexpr (PFIN == 0 && T % PFQ == 0 && T / PFQ < 3) {
                       if (w >= 2 && w <= 4) prefetch_lines(pfq, pf1 + (size_t)(T / PFQ + 1) * pfstep, (u32)(size_t)(lds + LDS_DUMP));
                     }
                     if constexpr (T == PFT) { if (w < 2) prefetch_lines(pfo, pfb, (u32)(size_t)(lds + LDS_DUMP)); } });
      bar();
      STAMP0(2);
      const int blk = (lane >> 4) & 1, q = (lane & 15) >> 2, p = lane & 3;
      bf16x8 wph[2];
#pragma unroll
      for (int t = 0; t < 2; ++t)
        wph[t] = lds_frag_tr(lds + LDS_WPHI + hh * 2048 + (16 * t + 4 * h + q) * 64 + (16 * blk + 4 * p) * 2, 8 * 64);
      f32x16 kv[2];
      zero16(kv[0]); zero16(kv[1]);
      float z[2] = {0.f, 0.f};
      LAS char* img = lds + 69632 + w * 4352;
      const u32 ones = 0x3f803f80u;
#pragma unroll
      for (int m = 0; m < 2; ++m) {
        f32x16 pp, ep, em;
        zero16(pp);
        pp = MFMA(wph[0], pack8(acc[m][1], 0), pp);
        pp = MFMA(wph[1], pack8(acc[m][1], 1), pp);
        dual_softmax(pp, ep, em);
#pragma unroll
        for (int g = 0; g < 4; ++g) {
          u32x2 a = {pk2(ep[4 * g], ep[4 * g + 1]), pk2(ep[4 * g + 2], ep[4 * g + 3])};
          u32x2 b = {pk2(em[4 * g], em[4 * g + 1]), pk2(em[4 * g + 2], em[4 * g + 3])};
          *(LAS u32x2*)(img + rho * 136 + (8 * g + 4 * h) * 2) = a;
          *(LAS u32x2*)(img + rho * 136 + (32 + 8 * g + 4 * h) * 2) = b;
        }
#pragma unroll
        for (int t = 0; t < 2; ++t) {
          const bf16x8 vf = pack8(acc[m][2], t);
#pragma unroll
          for (int F = 0; F < 2; ++F) {
            const bf16x8 af = lds_frag_tr(img + (16 * t + 4 * h + q) * 136 + (32 * F + 16 * blk + 4 * p) * 2, 8 * 136);
            kv[F] = MFMA(af, vf, kv[F]);
            const u32x4 au = __builtin_bit_cast(u32x4, af);
#pragma unroll
            for (int e = 0; e < 4; ++e) asm("v_dot2c_f32_bf16 %0, %1, %2" : "+v"(z[F]) : "v"(au[e]), "v"(ones));
          }
        }
      }
      {
        LAS char* red = lds + w * 8448;
#pragma unroll
        for (int F = 0; F < 2; ++F) {
#pragma unroll
          for (int g = 0; g < 4; ++g) {
            f32x4 v = {kv[F][4 * g], kv[F][4 * g + 1], kv[F][4 * g + 2], kv[F][4 * g + 3]};
            *(LAS f32x4*)(red + ((F * 4 + g) * 64 + lane) * 16) = v;
          }
          const float zt = xhalf_sum(z[F]);
          if (h == 0) *(LAS float*)(red + 8192 + (F * 32 + rho) * 4) = zt;
        }
      }
      wait_lgkm0();
      bar();
      const u32 kvp_me = (u32)(WS_KVP + (size_t)(hp * 8 + c) * KVP_BYTES);
      {
#pragma unroll
        for (int e = 0; e < 2; ++e) {
          f32x4 s = *(LAS f32x4*)(lds + e * 8448 + tid * 16);
#pragma unroll
          for (int k = 1; k < 4; ++k) s += *(LAS f32x4*)(lds + (2 * k + e) * 8448 + tid * 16);
          __builtin_amdgcn_raw_buffer_store_b128(__builtin_bit_cast(u32x4, s), wsr, kvp_me + e * 8448 + tid * 16, 0, 16);
        }
        if (tid < 32) {
          const int e = tid >> 4, u = 512 + (tid & 15);
          f32x4 s = *(LAS f32x4*)(lds + e * 8448 + u * 16);
#pragma unroll
          for (int k = 1; k < 4; ++k) s += *(LAS f32x4*)(lds + (2 * k + e) * 8448 + u * 16);
          __builtin_amdgcn_raw_buffer_store_b128(__builtin_bit_cast(u32x4, s), wsr, kvp_me + e * 8448 + u * 16, 0, 16);
        }
      }
      wait_vm<0>();
      bar();
      STAMP0(3);
#pragma unroll
      for (int m = 0; m < 2; ++m) {
        f32x16 pp, ep, em;
        zero16(pp);
        pp = MFMA(wph[0], pack8(acc[m][0], 0), pp);
        pp = MFMA(wph[1], pack8(acc[m][0], 1), pp);
        dual_softmax(pp, ep, em);
        phiq[m][0] = pack8(ep, 0); phiq[m][1] = pack8(ep, 1); phiq[m][2] = pack8(em, 0); phiq[m][3] = pack8(em, 1);
      }
    }
    bar();
    STAMP0(4);
    LAS char* kvb = lds + 73728;
    LAS char* zb = lds + 73728 + 8192;
    {
      const u32 kvp0 = (u32)(WS_KVP + (size_t)(hp * 8) * KVP_BYTES);
      f32x4 v[2][8];
#pragma unroll
      for (int e = 0; e < 2; ++e)
#pragma unroll
        for (int k = 0; k < 8; ++k)
          v[e][k] = __builtin_bit_cast(f32x4, __builtin_amdgcn_raw_buffer_load_b128(wsr, kvp0 + k * (u32)KVP_BYTES + e * 8448 + tid * 16, 0, 16));
      if (w == 7) {
        const int e = (lane >> 4) & 1, u = 512 + (lane & 15);
        f32x4 zv[4];
#pragma unroll
        for (int k = 0; k < 4; ++k)
          zv[k] = __builtin_bit_cast(f32x4, __builtin_amdgcn_raw_buffer_load_b128(wsr, kvp0 + (4 * h + k) * (u32)KVP_BYTES + e * 8448 + u * 16, 0, 16));
        f32x4 s = (zv[0] + zv[1]) + (zv[2] + zv[3]);
#pragma unroll
        for (int i = 0; i < 4; ++i) s[i] = xhalf_sum(s[i]);
        if (h == 0) *(LAS f32x4*)(zb + e * 256 + (lane & 15) * 16) = s;
      }
#pragma unroll
      for (int e = 0; e < 2; ++e) {
        f32x4 s = v[e][0];
#pragma unroll
        for (int k = 1; k < 8; ++k) s += v[e][k];
        u32x2 o = {pk2(s[0], s[1]), pk2(s[2], s[3])};
        *(LAS u32x2*)(kvb + e * 4096 + tid * 8) = o;
      }
    }
    wait_lgkm0();
    bar();
    STAMP0(9);
    {
      bf16x8 kvf[4];
      f32x4 zz[2][4];
#pragma unroll
      for (int F = 0; F < 2; ++F) {
#pragma unroll
        for (int t = 0; t < 2; ++t) {
          u32x2 a = *(LAS u32x2*)(kvb + hh * 4096 + ((F * 4 + 2 * t) * 64 + lane) * 8);
          u32x2 b = *(LAS u32x2*)(kvb + hh * 4096 + ((F * 4 + 2 * t + 1) * 64 + lane) * 8);
          u32x4 v = {a[0], a[1], b[0], b[1]};
          kvf[2 * F + t] = __builtin_bit_cast(bf16x8, v);
        }
#pragma unroll
        for (int g = 0; g < 4; ++g) zz[F][g] = *(LAS f32x4*)(zb + hh * 256 + (F * 32 + 8 * g + 4 * h) * 4);
      }
#pragma unroll
      for (int m = 0; m < 2; ++m) {
        f32x16 ot;
        zero16(ot);
        float den = 0.f;
#pragma unroll
        for (int i = 0; i < 4; ++i) {
          ot = MFMA(kvf[i], phiq[m][i], ot);
          const u32x4 pu = __builtin_bit_cast(u32x4, phiq[m][i]);
#pragma unroll
          for (int e = 0; e < 4; ++e) {
            const f32x4 zv = zz[i >> 1][2 * (i & 1) + (e >> 1)];
            den = fmaf(__uint_as_float(pu[e] << 16), zv[2 * (e & 1)], den);
            den = fmaf(__uint_as_float(pu[e] & 0xffff0000u), zv[2 * (e & 1) + 1], den);
          }
        }
        den = xhalf_sum(den) + 1e-6f;
        const float r = 1.0f / den;
        u32 d[8];
#pragma unroll
        for (int i = 0; i < 8; ++i) d[i] = pk2(ot[2 * i] * r, ot[2 * i + 1] * r);
        const u32 obase = (u32)(WS_O + (size_t)((c * 8 + wm * 2 + m) * 16 + hp) * 4096);
#pragma unroll
        for (int i = 0; i < 2; ++i) {
          auto r0 = __builtin_amdgcn_permlane32_swap(d[4 * i], d[4 * i + 2], false, false);
          auto r1 = __builtin_amdgcn_permlane32_swap(d[4 * i + 1], d[4 * i + 3], false, false);
          u32x4 v = {r0[0], r1[0], r0[1], r1[1]};
          __builtin_amdgcn_raw_buffer_store_b128(v, wsr, obase + ((4 * hh + 2 * i + h) * 32 + rho) * 16, 0, 16);
        }
      }
    }
    wait_vm<0>();
    wait_lgkm0();
    bar();
    STAMP0(5);
    f32x16 acc[2][2];
    zero16(acc[0][0]); zero16(acc[0][1]); zero16(acc[1][0]); zero16(acc[1][1]);
    p2_mma<true>(lds, acc, w, lane, [&]() { STAMP0(6); });
    STAMP0(7);
    p2_combine(lds, acc, w, lane);
    const int kh = w >> 2, wx = (w >> 1) & 1, wy = w & 1;
    float* op = P.out + (size_t)(rb * 128 + wx * 64 + kh * 32 + 4 * h) * 2048 + nb * 128 + wy * 64 + rho;
#pragma unroll
    for (int r = 0; r < 16; ++r)
#pragma unroll
      for (int j = 0; j < 2; ++j) op[((r & 3) + 8 * (r >> 2)) * 2048 + 32 * j] = acc[0][j][r];
    STAMP0(8);
  } else {
    const int wm = w >> 1, pn = w & 1, pg = cg, pl = pn * 32 + rho;
    {
      f32x16 acc[2][2];
      zero16(acc[0][0]); zero16(acc[0][1]); zero16(acc[1][0]); zero16(acc[1][1]);
      const int gb = xcd * 16 + li;
      const u32 pfq = (u32)((lane >> 2) * 2048 + (lane & 3) * 128);
      const char* pf1 = w == 4 ? (const char*)P.Bx + (size_t)gb * (16 * 2048)
                               : (const char*)((gb & 64) ? P.bim : P.bre) + (size_t)(gb & 63) * (16 * 2048);
      if (w == 7) {
        const int pidx = pg * 64 + lane;
        const float la = bf2f(P.lre[pidx]), lb = bf2f(P.lim[pidx]), ldt = bf2f(P.logdt[pidx]);
        const float dt = bf2f((u16)(pk2(expf(ldt), 0.f) & 0xffffu));
        const float er = expf(dt * la);
        float sn, cs;
        sincosf(dt * lb, &sn, &cs);
        const float lr = er * cs, li_ = er * sn;
        const float nr = lr - 1.0f, dn = 1.0f / (la * la + lb * lb);
        const float c2r = 2.0f * (nr * la + li_ * lb) * dn, c2i = 2.0f * (li_ * la - nr * lb) * dn;
        float pr = lr, pi = li_;
#pragma unroll
        for (int i = 0; i < 5; ++i) { const float a = pr * pr - pi * pi, b = 2.0f * pr * pi; pr = a; pi = b; }
        *(LAS f32x4*)(lds + LDS_CONST + lane * 32) = (f32x4){lr, li_, c2r, c2i};
        *(LAS f32x2*)(lds + LDS_CONST + lane * 32 + 16) = (f32x2){pr, pi};
        wait_lgkm0();
      }
      const u32 pfo = (u32)(lane * 128);
      const char* pfb = (const char*)((w & 2) ? P.cim : P.cre) + (size_t)((xcd * 16 + li) * 2 + (w & 1)) * 8192;
      p1_mma<false>(lds, acc, w, lane, [&]() { STAMP0(1); },
                    [&](auto T_) {
                      constexpr int T = decltype(T_)::value;
                      if constexpr (PFIN == 0 && T % PFQ == 0 && T / PFQ < 3) {
                        if (w == 4 || w == 5) prefetch_lines(pfq, pf1 + (T / PFQ + 1) * 512, (u32)(size_t)(lds + LDS_DUMP));
                      }
                      if constexpr (decltype(T_)::value == PFT) { if (w < 4) prefetch_lines(pfo, pfb, (u32)(size_t)(lds + LDS_DUMP)); } });
      STAMP0(2);
      const f32x4 k0 = *(LAS f32x4*)(lds + LDS_CONST + pl * 32);
      const f32x2 k1 = *(LAS f32x2*)(lds + LDS_CONST + pl * 32 + 16);
      const float lr = k0[0], li_ = k0[1], c2r = k0[2], c2i = k0[3], pr = k1[0], pi = k1[1];
      float gr = 0.f, gi = 0.f;
#pragma unroll
      for (int m = 0; m < 2; ++m)
#pragma unroll
        for (int r = 0; r < 16; ++r) {
          const float a = fmaf(lr, gr, fmaf(-li_, gi, acc[m][0][r]));
          const float b = fmaf(lr, gi, fmaf(li_, gr, acc[m][1][r]));
          gr = a; gi = b;
        }
      const int sg = 2 * wm + h;
      LAS f32x2* E = (LAS f32x2*)(lds + LDS_E);
      E[sg * 64 + pl] = (f32x2){gr, gi};
      wait_lgkm0();
      bar();
      STAMP0(3);
      float qr = 0.f, qi = 0.f, wr_ = 1.f, wi_ = 0.f;
#pragma unroll
      for (int k = 0; k < 7; ++k) {
        const f32x2 e = E[k * 64 + pl];
        const float a = fmaf(pr, qr, fmaf(-pi, qi, e[0])), b = fmaf(pr, qi, fmaf(pi, qr, e[1]));
        const float a2 = wr_ * pr - wi_ * pi, b2 = wr_ * pi + wi_ * pr;
        const bool take = k < sg;
        qr = take ? a : qr; qi = take ? b : qi; wr_ = take ? a2 : wr_; wi_ = take ? b2 : wi_;
      }
      bar();
      STAMP0(4);
      {
        const f32x2 hin = *(LAS f32x2*)(lds + LDS_HIN + pl * 8);
        gr = fmaf(wr_, hin[0], fmaf(-wi_, hin[1], qr));
        gi = fmaf(wr_, hin[1], fmaf(wi_, hin[0], qi));
      }
      const u32 hbase = (u32)(WS_H + (size_t)((((c * 4 + wm) * 16 + pg) * 2) * 2 + pn) * 4096) + (u32)(((rho >> 2) * 8 + 4 * h) * 64 + (rho & 3) * 16);
#pragma unroll
      for (int m = 0; m < 2; ++m)
#pragma unroll
        for (int half = 0; half < 2; ++half) {
          float hr[8], hi[8];
#pragma unroll
          for (int r = 0; r < 8; ++r) {
            const float a = fmaf(lr, gr, fmaf(-li_, gi, acc[m][0][8 * half + r]));
            const float b = fmaf(lr, gi, fmaf(li_, gr, acc[m][1][8 * half + r]));
            gr = a; gi = b;
            hr[r] = c2r * a - c2i * b;
            hi[r] = -(c2r * b + c2i * a);
          }
          u32x4 vr = {pk2(hr[0], hr[1]), pk2(hr[2], hr[3]), pk2(hr[4], hr[5]), pk2(hr[6], hr[7])};
          u32x4 vi = {pk2(hi[0], hi[1]), pk2(hi[2], hi[3]), pk2(hi[4], hi[5]), pk2(hi[6], hi[7])};
          const u32 o = hbase + (2 * m + half) * 64;
          __builtin_amdgcn_raw_buffer_store_b128(vr, wsr, o, 0, 16);
          __builtin_amdgcn_raw_buffer_store_b128(vi, wsr, o + 8192, 0, 16);
        }
    }
    wait_vm<0>();
    wait_lgkm0();
    bar();
    STAMP0(5);
    const int kh = w >> 2, wx = (w >> 1) & 1, wy = w & 1;
    const int d = nb * 128 + wx * 64 + kh * 32 + rho, t0 = rb * 128 + wy * 64 + 4 * h;
    f32x16 acc[2][2];
    {
      const u16* xp = P.Bx + (size_t)t0 * 1024 + d;
      const float dsk = bf2f(P.dskip[d]);
#pragma unroll
      for (int j = 0; j < 2; ++j)
#pragma unroll
        for (int r = 0; r < 16; ++r) {
          const float v = bf2f(xp[(32 * j + (r & 3) + 8 * (r >> 2)) * 1024]) * dsk;
          acc[0][j][r] = kh ? 0.f : v;
          acc[1][j][r] = kh ? v : 0.f;
        }
    }
    p2_mma<false>(lds, acc, w, lane, [&]() { STAMP0(6); });
    STAMP0(7);
    p2_combine(lds, acc, w, lane);
    float* op = P.out + (size_t)t0 * 2048 + 1024 + d;
#pragma unroll
    for (int j = 0; j < 2; ++j)
#pragma unroll
      for (int r = 0; r < 16; ++r) op[(32 * j + (r & 3) + 8 * (r >> 2)) * 2048] = acc[0][j][r];
    STAMP0(8);
  }
}

extern "C" void kernel_launch(void* const* d_in, const int* in_sizes, int n_in,
                              void* d_out, int out_size, void* d_ws, size_t ws_size,
                              hipStream_t stream) {
  Params P;
  P.Ax = (const u16*)d_in[0]; P.wqkv = (const u16*)d_in[1]; P.wphi = (const u16*)d_in[2]; P.wo = (const u16*)d_in[3];
  P.Bx = (const u16*)d_in[4]; P.lre = (const u16*)d_in[5]; P.lim = (const u16*)d_in[6];
  P.bre = (const u16*)d_in[7]; P.bim = (const u16*)d_in[8]; P.cre = (const u16*)d_in[9]; P.cim = (const u16*)d_in[10];
  P.logdt = (const u16*)d_in[11]; P.dskip = (const u16*)d_in[12];
  P.out = (float*)d_out; P.ws = (char*)d_ws;
  (void)hipFuncSetAttribute((const void*)fused_kernel, hipFuncAttributeMaxDynamicSharedMemorySize, LDS_BYTES);
  fused_kernel<<<256, 768, LDS_BYTES, stream>>>(P);
}
```

```cpp
#include <hip/hip_runtime.h>
#include <hip/hip_bf16.h>

typedef unsigned short u16;
typedef unsigned int u32;
typedef unsigned long long u64;
typedef __bf16 bf16x8 __attribute__((ext_vector_type(8)));
typedef __bf16 bf16x2 __attribute__((ext_vector_type(2)));
typedef short s16x4 __attribute__((ext_vector_type(4)));
typedef short s16x8 __attribute__((ext_vector_type(8)));
typedef float f32x16 __attribute__((ext_vector_type(16)));
typedef float f32x4 __attribute__((ext_vector_type(4)));
typedef float f32x2 __attribute__((ext_vector_type(2)));
typedef u32 u32x4 __attribute__((ext_vector_type(4)));
typedef u32 u32x2 __attribute__((ext_vector_type(2)));
#define LAS __attribute__((address_space(3)))
#define GAS __attribute__((address_space(1)))
#define DI __device__ __forceinline__
#define RLX_AGENT __ATOMIC_RELAXED, __HIP_MEMORY_SCOPE_AGENT

constexpr size_t WS_EPOCH_A = 0;
constexpr size_t WS_EPOCH_B = 256;
constexpr size_t WS_KVFLAG = 4096;
constexpr size_t WS_OFLAG = WS_KVFLAG + 16 * 128;
constexpr size_t WS_HFLAG = WS_OFLAG + 8 * 128;
constexpr size_t WS_AGG = 65536;
constexpr size_t WS_KVP = WS_AGG + 16 * 8 * 1024;
constexpr size_t KVP_BYTES = (1024 + 64) * 16;
constexpr size_t WS_O = 4u << 20;
constexpr size_t WS_H = 8u << 20;
constexpr size_t WS_END = 16u << 20;

constexpr int LDS_BYTES = 163840;

struct Params {
  const u16* Ax; const u16* wqkv; const u16* wphi; const u16* wo;
  const u16* Bx; const u16* lre; const u16* lim; const u16* bre; const u16* bim;
  const u16* cre; const u16* cim; const u16* logdt; const u16* dskip;
  float* out; char* ws;
};

DI float bf2f(u16 v) { return __uint_as_float(((u32)v) << 16); }
DI u32 pk2(float a, float b) {
  f32x2 v = {a, b};
  bf16x2 r = __builtin_convertvector(v, bf16x2);
  return __builtin_bit_cast(u32, r);
}
DI bf16x8 pack8(const f32x16& x, int s) {
  u32x4 p;
  p[0] = pk2(x[8 * s + 0], x[8 * s + 1]); p[1] = pk2(x[8 * s + 2], x[8 * s + 3]);
  p[2] = pk2(x[8 * s + 4], x[8 * s + 5]); p[3] = pk2(x[8 * s + 6], x[8 * s + 7]);
  return __builtin_bit_cast(bf16x8, p);
}
#define MFMA(a, b, c) __builtin_amdgcn_mfma_f32_32x32x16_bf16((a), (b), (c), 0, 0, 0)
#define LMFMA MFMA
DI bf16x8 lds_frag(LAS char* p) { return *(LAS bf16x8*)p; }
DI s16x4 lds_tr(LAS char* __restrict__ p) { return __builtin_amdgcn_ds_read_tr16_b64_v4i16((LAS s16x4*)p); }
DI bf16x8 lds_frag_tr(LAS char* p, int off2) {
  s16x4 lo = lds_tr(p), hi = lds_tr(p + off2);
  s16x8 v = __builtin_shufflevector(lo, hi, 0, 1, 2, 3, 4, 5, 6, 7);
  return __builtin_bit_cast(bf16x8, v);
}
template <int OFF = 0> DI void dma_s(u32 voff, const char* sbase, u32 ldsaddr) {
  asm volatile("s_mov_b32 m0, %2\n\ts_nop 0\n\tglobal_load_lds_dwordx4 %0, %1 offset:%3" ::"v"(voff), "s"(sbase), "s"(ldsaddr), "n"(OFF) : "memory");
}
DI void dma_s4_dev(u32 voff, const char* sbase, u32 ldsaddr) {
  asm volatile("s_mov_b32 m0, %2\n\ts_nop 0\n\tglobal_load_lds_dwordx4 %0, %1 sc1\n\tglobal_load_lds_dwordx4 %0, %1 offset:1024 sc1\n\t"
               "global_load_lds_dwordx4 %0, %1 offset:2048 sc1\n\tglobal_load_lds_dwordx4 %0, %1 offset:3072 sc1"
               ::"v"(voff), "s"(sbase), "s"(ldsaddr) : "memory");
}
template <int N> DI void wait_vm() { asm volatile("s_waitcnt vmcnt(%0)" ::"n"(N) : "memory"); }
DI void wait_lgkm0() { asm volatile("" ::: "memory"); __builtin_amdgcn_s_waitcnt(0xC07F); asm volatile("" ::: "memory"); }
#define SB() __builtin_amdgcn_sched_barrier(0)
DI void bar() { asm volatile("" ::: "memory"); __builtin_amdgcn_s_barrier(); asm volatile("" ::: "memory"); }
DI float xhalf_max(float v) {
  auto r = __builtin_amdgcn_permlane32_swap(__float_as_uint(v), __float_as_uint(v), false, false);
  return fmaxf(__uint_as_float(r[0]), __uint_as_float(r[1]));
}
DI float xhalf_min(float v) {
  auto r = __builtin_amdgcn_permlane32_swap(__float_as_uint(v), __float_as_uint(v), false, false);
  return fminf(__uint_as_float(r[0]), __uint_as_float(r[1]));
}
DI float xhalf_sum(float v) {
  auto r = __builtin_amdgcn_permlane32_swap(__float_as_uint(v), __float_as_uint(v), false, false);
  return __uint_as_float(r[0]) + __uint_as_float(r[1]);
}
DI u64 next_tag(u64 e) { return e * 6364136223846793005ull + 1442695040888963407ull; }

DI void wait_flags(const char* flags, int n, u64 tag, int lane) {
  const GAS u64* f = (const GAS u64*)flags + (lane < n ? lane : 0);
  const u64 t0 = __builtin_amdgcn_s_memrealtime();
  for (;;) {
    u64 v = __hip_atomic_load(f, RLX_AGENT);
    if (__all(v == tag)) break;
    if (__builtin_amdgcn_s_memrealtime() - t0 > 500000ull) break;
    __builtin_amdgcn_s_sleep(1);
  }
}

#pragma float_control(precise, off, push)
DI void dual_softmax(const f32x16& p, u32 (&op)[8], u32 (&om)[8]) {
  float mx = fmaxf(p[0], p[1]), mn = fminf(p[0], p[1]);
#pragma unroll
  for (int i = 2; i < 16; i += 2) { mx = fmaxf(fmaxf(mx, p[i]), p[i + 1]); mn = fminf(fminf(mn, p[i]), p[i + 1]); }
  mx = xhalf_max(mx); mn = xhalf_min(mn);
  const float L2E = 1.4426950408889634f;
  const f32x2 cp = {L2E, L2E}, cm = {-L2E, -L2E};
  const f32x2 bp = {-mx * L2E, -mx * L2E}, bm = {mn * L2E, mn * L2E};
  f32x2 ep[8], em[8];
#pragma unroll
  for (int i = 0; i < 8; ++i) {
    const f32x2 v = {p[2 * i], p[2 * i + 1]};
    const f32x2 a = __builtin_elementwise_fma(v, cp, bp), b = __builtin_elementwise_fma(v, cm, bm);
    ep[i] = (f32x2){__builtin_amdgcn_exp2f(a[0]), __builtin_amdgcn_exp2f(a[1])};
    em[i] = (f32x2){__builtin_amdgcn_exp2f(b[0]), __builtin_amdgcn_exp2f(b[1])};
  }
  const f32x2 s2p = ((ep[0] + ep[1]) + (ep[2] + ep[3])) + ((ep[4] + ep[5]) + (ep[6] + ep[7]));
  const f32x2 s2m = ((em[0] + em[1]) + (em[2] + em[3])) + ((em[4] + em[5]) + (em[6] + em[7]));
  const float rp = __builtin_amdgcn_rcpf(xhalf_sum(s2p[0] + s2p[1])), rm = __builtin_amdgcn_rcpf(xhalf_sum(s2m[0] + s2m[1]));
  const f32x2 rp2 = {rp, rp}, rm2 = {rm, rm};
#pragma unroll
  for (int i = 0; i < 8; ++i) {
    const f32x2 a = ep[i] * rp2, b = em[i] * rm2;
    op[i] = pk2(a[0], a[1]); om[i] = pk2(b[0], b[1]);
  }
}
#pragma float_control(pop)
DI bf16x8 frag4(u32 a, u32 b, u32 c, u32 d) { const u32x4 v = {a, b, c, d}; return __builtin_bit_cast(bf16x8, v); }

#define FRONT -1
template <int NM, int ND> DI void interleave() {
  constexpr int FR = FRONT > 0 ? FRONT : NM + FRONT;
  constexpr int DPM = (ND + FR - 1) / FR;
#pragma unroll
  for (int i = 0; i < NM; ++i) {
    __builtin_amdgcn_sched_group_barrier(0x008, 1, 0);
    if (i * DPM < ND) __builtin_amdgcn_sched_group_barrier(0x100, DPM, 0);
  }
}
#define IC(n) std::integral_constant<int, (n)>{}
constexpr int LDS_DUMP = 163584;
DI void prefetch_lines(u32 voff, const char* sbase, u32 dump) {
  asm volatile("s_mov_b32 m0, %2\n\ts_nop 0\n\tglobal_load_lds_dword %0, %1" ::"v"(voff), "s"(sbase), "s"(dump) : "memory");
}

constexpr int P1_WOFF = 98304;
#define HINT_SLEEP 4
#define SPEC 40
#define OUT_AUX 16
#define GATHER_AUX 16
constexpr u32 CODE_PF_LINES = 52;
#define PFC 99
#define PFT 6
#define PFIN 0
#define PFD 5
#define ROTA 0
#define ROTB 0
#define WPF 0
#define PFINB 0
#define ROT2 1

template <int N, class F, int I = 0> DI void static_for(F&& f) {
  if constexpr (I < N) { f(std::integral_constant<int, I>{}); static_for<N, F, I + 1>(static_cast<F&&>(f)); }
}

template <bool ISA, class HOOK>
DI void p1_load(u32 ldsb, const char* xg, const char* wg0, const char* wg1, int lw, int lane, int rot, HOOK&& hook, u32* ttout = nullptr) {
  constexpr int WSLOT = ISA ? 12288 : 16384, NW = ISA ? 5 : 3;
  const u32 offE = (u32)((lane >> 3) * 2048 + (((lane & 7) ^ (lane >> 4)) * 16));
  const u32 offO = (u32)((8 + (lane >> 3)) * 2048 + (((lane & 7) ^ (4 + (lane >> 4))) * 16));
  const u32 offK = (u32)((lane >> 3) * 6144 + (((lane & 7) ^ (((lane >> 4) & 1) << 2)) * 16));
  const char* xb = xg + lw * (64 * 2048);
  const char* wb = ISA ? wg0 + lw * (8 * 6144) : (lw < 2 ? wg0 : wg1) + (lw & 1) * (32 * 2048);
  auto issue_x = [&](int T, int so) {
    const u32 l = ldsb + so + lw * 8192;
    const char* g = xb + ((T + rot) & 15) * 128;
#pragma unroll
    for (int a = 0; a < 4; ++a) { dma_s(offE, g + a * 32768, l + a * 2048); dma_s(offO, g + a * 32768, l + a * 2048 + 1024); }
  };
  auto issue_w = [&](int u, int so) {
    if constexpr (ISA) {
      const u32 l = ldsb + P1_WOFF + so + lw * 1024;
      const char* g = wb + (size_t)((u + 2 * rot) & 31) * (32 * 6144);
      dma_s(offK, g, l); dma_s(offK, g + 2048, l + 4096); dma_s(offK, g + 4096, l + 8192);
    } else {
      const u32 l = ldsb + P1_WOFF + so + lw * 4096;
      const char* g = wb + ((u + rot) & 15) * 128;
      dma_s(offE, g, l); dma_s(offO, g, l + 1024); dma_s(offE, g + 32768, l + 2048); dma_s(offO, g + 32768, l + 3072);
    }
  };
  auto wnext = [&](int o) { return o + WSLOT == NW * WSLOT ? 0 : o + WSLOT; };
  auto xnext = [&](int o) { return o == 65536 ? 0 : o + 32768; };
  int xso = 0, wso = 0;
  if constexpr (ISA) {
    issue_w(0, 0); issue_x(0, 0); issue_w(1, WSLOT);
    wait_vm<3>(); bar();
    issue_w(2, 2 * WSLOT); issue_x(1, 32768); issue_w(3, 3 * WSLOT); issue_w(4, 4 * WSLOT); issue_x(2, 65536);
#pragma unroll 1
    for (int T = 0; T < 13; ++T) {
      const int ws1 = wnext(wso);
      wait_vm<9 + 16>();
      bar();
      issue_w(2 * T + 5, wso);
      wait_vm<9 + 8>(); bar();
      issue_w(2 * T + 6, ws1); issue_x(T + 3, xso);
      wso = wnext(ws1); xso = xnext(xso);
    }
    wait_vm<9 + 16>(); bar();
    issue_w(31, wso);
    wait_vm<9 + 8>(); bar();
    hook(IC(0));
    wait_vm<6 + 8 + 1>(); bar();
    wait_vm<3 + 1>(); bar();
    wait_vm<0>(); bar();
  } else {
    issue_w(0, 0); issue_x(0, 0); issue_w(1, WSLOT); issue_x(1, 32768);
    wait_vm<12>(); bar();
    issue_w(2, 2 * WSLOT); issue_x(2, 65536);
#pragma unroll 1
    for (int T = 0; T < 13; ++T) {
      wait_vm<12>();
      bar();
      issue_w(T + 3, wso); issue_x(T + 3, xso);
      wso = wnext(wso); xso = xnext(xso);
    }
    wait_vm<12>(); bar();
    wait_vm<0>(); bar();
  }
}

template <bool ISA, class ST, class PF>
DI void p1_mma(LAS char* lds, f32x16 (&acc)[2][ISA ? 3 : 2], int w, int lane, ST&& st, PF&& pf) {
  constexpr int NT = ISA ? 3 : 2;
  constexpr int WSLOT = ISA ? 12288 : 16384;
  constexpr int NM = 2 * NT, ND = ISA ? 8 : 4;
  const int wm = w >> 1, wn = w & 1, h = lane >> 5, rho = lane & 31;
  const int arow = wm * 64 + (ISA ? rho : (32 * ((rho >> 2) & 1) + (rho & 3) + 4 * (rho >> 3)));
  constexpr int AM = ISA ? 32 * 128 : 16 * 128;
  const int aoff = arow * 128 + ((h ^ ((arow >> 1) & 7)) * 16);
  int boff;
  if constexpr (ISA) {
    const int blk = (lane >> 4) & 1, q = (lane & 15) >> 2, p = lane & 3;
    const int c = (4 * wn + 2 * blk + (p >> 1)) ^ (((q >> 1) & 1) << 2);
    boff = P1_WOFF + (8 * h + q) * 128 + c * 16 + 8 * (p & 1);
  } else {
    const int brow = wn * 32 + rho;
    boff = P1_WOFF + brow * 128 + ((h ^ ((brow >> 1) & 7)) * 16);
  }
  LAS char* pa[2][4];
  LAS char* pb[4];
#pragma unroll
  for (int s4 = 0; s4 < 4; ++s4) {
    pa[0][s4] = lds + (aoff ^ (s4 * 32)); pa[1][s4] = pa[0][s4] + 65536;
    pb[s4] = lds + (ISA ? boff : (boff ^ (s4 * 32)));
    asm volatile("" : "+v"(pa[0][s4]), "+v"(pa[1][s4]));
    if (!ISA || s4 == 0) asm volatile("" : "+v"(pb[s4]));
  }
  bf16x8 fa0[2], fb0[NT], fa1[2], fb1[NT];
  auto rd = [&](bf16x8 (&fa)[2], bf16x8 (&fb)[NT], auto T_, auto s4_) {
    constexpr int T = decltype(T_)::value, s4 = decltype(s4_)::value;
    constexpr int xs = T % 3;
    LAS char* a = pa[xs >> 1][s4] + (xs & 1) * 32768;
    fa[0] = lds_frag(a); fa[1] = lds_frag(a + AM);
    if constexpr (ISA) {
      LAS char* b = pb[0] + ((2 * T + (s4 >> 1)) % 5) * WSLOT + (s4 & 1) * 2048;
#pragma unroll
      for (int j = 0; j < 3; ++j) fb[j] = lds_frag_tr(b + j * 4096, 512);
    } else {
      LAS char* b = pb[s4] + xs * WSLOT;
      fb[0] = lds_frag(b); fb[1] = lds_frag(b + 64 * 128);
    }
  };
  auto mm = [&](bf16x8 (&fa)[2], bf16x8 (&fb)[NT]) {
#pragma unroll
    for (int m = 0; m < 2; ++m) {
      if constexpr (ISA) {
        acc[m][0] = LMFMA(fb[0], fa[m], acc[m][0]);
        acc[m][1] = LMFMA(fb[1], fa[m], acc[m][1]);
        acc[m][2] = LMFMA(fa[m], fb[2], acc[m][2]);
      } else {
        acc[m][0] = LMFMA(fa[m], fb[0], acc[m][0]);
        acc[m][1] = LMFMA(fa[m], fb[1], acc[m][1]);
      }
    }
  };
  bar();
  st();
  rd(fa0, fb0, IC(0), IC(0));
  static_for<16>([&](auto T_) {
    constexpr int T = decltype(T_)::value;
    rd(fa1, fb1, T_, IC(1)); mm(fa0, fb0); interleave<NM, ND>(); SB();
    if constexpr (ISA) { wait_lgkm0(); bar(); }
    rd(fa0, fb0, T_, IC(2)); mm(fa1, fb1); interleave<NM, ND>(); SB();
    pf(T_);
    rd(fa1, fb1, T_, IC(3)); mm(fa0, fb0); interleave<NM, ND>(); SB();
    if constexpr (T < 15) { wait_lgkm0(); bar(); rd(fa0, fb0, IC(T + 1), IC(0)); }
    mm(fa1, fb1); interleave<NM, T < 15 ? ND : 0>(); SB();
  });
}

template <bool ISA>
DI void p2_load(u32 ldsb, const char* xg0, const char* xg1, const char* yg, int lw, int lane,
                int rot, const char* flags, u64 tag, GAS u64* epoch_to_bump) {
  constexpr int NS = 4, STAGE = 32768, NK = ISA ? 16 : 32;
  const u32 lin = (u32)(lane * 16);
  u32 swE, swO;
  const char *wb0, *wb1, *hb;
  if constexpr (ISA) {
    swE = (u32)((lane >> 3) * 2048 + (((lane & 7) ^ (((lane >> 4) & 1) << 2)) * 16));
    swO = swE + 8 * 2048;
    wb0 = wb1 = yg + (lw & 1) * (32 * 2048) + (lw >> 1) * 128;
    hb = xg0 + lw * (16 * 4096);
  } else {
    swE = (u32)((lane >> 3) * 2048 + (((lane & 7) ^ (lane >> 4)) * 16));
    swO = (u32)((8 + (lane >> 3)) * 2048 + (((lane & 7) ^ (4 + (lane >> 4))) * 16));
    wb0 = xg0 + lw * (32 * 2048); wb1 = xg1 + lw * (32 * 2048);
    hb = yg + (lw >> 1) * (64 * 4096) + (lw & 1) * 4096;
  }
  auto issue_w = [&](int st, int so) {
    st = (st + rot) & (NK - 1);
    const u32 l = ldsb + so + (ISA ? 16384 : 0) + lw * 4096;
    const char* g = ISA ? wb0 + (size_t)st * (64 * 2048) : ((st & 1) ? wb1 : wb0) + (st >> 1) * 128;
    dma_s(swE, g, l); dma_s(swO, g, l + 1024); dma_s(swE, g + 32768, l + 2048); dma_s(swO, g + 32768, l + 3072);
  };
  auto issue_h = [&](int st, int so) {
    st = (st + rot) & (NK - 1);
    dma_s4_dev(lin, hb + st * (ISA ? 4096 : 8192), ldsb + so + (ISA ? 0 : 16384) + lw * 4096);
  };
#pragma unroll
  for (int s = 0; s < NS; ++s) issue_w(s, s * STAGE);
  if (lw == 0) {
    wait_flags(flags, 16, tag, lane);
    if (epoch_to_bump != nullptr && lane == 0) __hip_atomic_store(epoch_to_bump, tag, RLX_AGENT);
  }
  bar();
#pragma unroll
  for (int s = 0; s < NS; ++s) issue_h(s, s * STAGE);
  wait_vm<12>(); bar();
  int so = 0;
  auto nxt = [&]() { so = so == (NS - 1) * STAGE ? 0 : so + STAGE; };
  wait_vm<8>(); bar(); issue_h(NS + 0, so); issue_w(NS + 0, so); nxt();
  wait_vm<12>(); bar(); issue_h(NS + 1, so); issue_w(NS + 1, so); nxt();
#pragma unroll 1
  for (int t = 2; t < NK - NS; ++t) { wait_vm<16>(); bar(); issue_h(t + NS, so); issue_w(t + NS, so); nxt(); }
  wait_vm<16>(); bar(); wait_vm<8>(); bar(); wait_vm<0>(); bar();
  bar(); bar();
}

template <bool ISA, class ST>
DI void p2_mma(LAS char* lds, f32x16 (&acc)[2][2], int w, int lane, ST&& st) {
  constexpr int NS = 4, STAGE = 32768, NK = ISA ? 16 : 32;
  const int kh = w >> 2, wx = (w >> 1) & 1, wy = w & 1, h = lane >> 5, rho = lane & 31;
  const int blk = (lane >> 4) & 1, q = (lane & 15) >> 2, p = lane & 3;
  int xo, yo;
  if constexpr (ISA) {
    xo = wx * 8192 + kh * 2048 + h * 512 + rho * 16;
    yo = 16384 + wy * 8192 + (32 * kh + 8 * h + q) * 128 + (((2 * blk + (p >> 1)) ^ (((q >> 1) & 1) << 2)) * 16) + 8 * (p & 1);
  } else {
    const int xrow = wx * 64 + rho;
    xo = xrow * 128 + (((4 * kh + h) ^ ((xrow >> 1) & 7)) * 16);
    yo = 16384 + wy * 8192 + kh * 4096 + (2 * h * 8 + 2 * blk + (p >> 1)) * 64 + q * 16 + 8 * (p & 1);
  }
  LAS char* px[2][2];
  LAS char* py[2][2];
#pragma unroll
  for (int wdw = 0; wdw < 2; ++wdw) {
    px[wdw][0] = lds + wdw * 65536 + xo; px[wdw][1] = lds + wdw * 65536 + (xo ^ 32);
    py[wdw][0] = lds + wdw * 65536 + yo; py[wdw][1] = lds + wdw * 65536 + (yo ^ 64);
    asm volatile("" : "+v"(px[wdw][0]), "+v"(py[wdw][0]));
    if (ISA) asm volatile("" : "+v"(py[wdw][1])); else asm volatile("" : "+v"(px[wdw][1]));
  }
  bf16x8 fx0[2], fy0[2], fx1[2], fy1[2];
  auto rd = [&](bf16x8 (&fx)[2], bf16x8 (&fy)[2], auto slot_, auto step_) {
    constexpr int slot = decltype(slot_)::value, step = decltype(step_)::value;
    constexpr int wdw = slot >> 1, so = (slot & 1) * STAGE;
    if constexpr (ISA) {
      LAS char* x = px[wdw][0] + so + step * 1024;
      fx[0] = lds_frag(x); fx[1] = lds_frag(x + 4096);
      fy[0] = lds_frag_tr(py[wdw][0] + so + step * 2048, 512);
      fy[1] = lds_frag_tr(py[wdw][1] + so + step * 2048, 512);
    } else {
      LAS char* x = px[wdw][step] + so;
      fx[0] = lds_frag(x); fx[1] = lds_frag(x + 32 * 128);
      fy[0] = lds_frag_tr(py[wdw][0] + so + step * 2048, 512);
      fy[1] = lds_frag_tr(py[wdw][0] + so + step * 2048 + 256, 512);
    }
  };
  auto mm = [&](bf16x8 (&fx)[2], bf16x8 (&fy)[2]) {
#pragma unroll
    for (int i = 0; i < 2; ++i)
#pragma unroll
      for (int j = 0; j < 2; ++j)
        acc[i][j] = ISA ? LMFMA(fy[j], fx[i], acc[i][j]) : LMFMA(fx[i], fy[j], acc[i][j]);
  };
  bar();
  bar();
  st();
  rd(fx0, fy0, IC(0), IC(0));
  auto stage = [&](auto slot_, auto last_) {
    constexpr int slot = decltype(slot_)::value;
    constexpr bool LAST = decltype(last_)::value;
    rd(fx1, fy1, slot_, IC(1)); mm(fx0, fy0); interleave<4, 6>(); SB();
    if constexpr (!LAST) { wait_lgkm0(); bar(); rd(fx0, fy0, IC((slot + 1) % NS), IC(0)); }
    mm(fx1, fy1); interleave<4, LAST ? 0 : 6>(); SB();
  };
#pragma unroll 1
  for (int t = 0; t < NK / NS - 1; ++t) {
    stage(IC(0), std::false_type{}); stage(IC(1), std::false_type{}); stage(IC(2), std::false_type{}); stage(IC(3), std::false_type{});
  }
  stage(IC(0), std::false_type{}); stage(IC(1), std::false_type{}); stage(IC(2), std::false_type{}); stage(IC(3), std::true_type{});
}

template <bool ISA>
DI void p2_store(LAS char* lds, const f32x16 (&acc)[2][2], int w, int lane, __amdgpu_buffer_rsrc_t outr, u32 out_off, u32x2 (&xv)[8], u32x2 dsk) {
  constexpr int PITCH = 528, HALF = 128 * PITCH;
  const int kh = w >> 2, wx = (w >> 1) & 1, wy = w & 1, h = lane >> 5, rho = lane & 31;
  bar();
  LAS char* base = lds + kh * HALF + ((ISA ? wx : wy) * 64 + rho) * PITCH + ((ISA ? wy : wx) * 64 + 4 * h) * 4;
#pragma unroll
  for (int i = 0; i < 2; ++i)
#pragma unroll
    for (int j = 0; j < 2; ++j)
#pragma unroll
      for (int g = 0; g < 4; ++g) {
        const f32x4 v = {acc[i][j][4 * g], acc[i][j][4 * g + 1], acc[i][j][4 * g + 2], acc[i][j][4 * g + 3]};
        *(LAS f32x4*)(base + (ISA ? i : j) * (32 * PITCH) + ((ISA ? j : i) * 32 + 8 * g) * 4) = v;
      }
  wait_lgkm0();
  bar();
  if (!ISA) {
#pragma unroll
    for (int i = 0; i < 8; ++i) asm volatile("" : "+v"(xv[i]));
  }
  const f32x4 dv = {__uint_as_float(dsk[0] << 16), __uint_as_float(dsk[0] & 0xffff0000u), __uint_as_float(dsk[1] << 16), __uint_as_float(dsk[1] & 0xffff0000u)};
  LAS char* rp = lds + (w * 16 + h) * PITCH + rho * 16;
  const u32 vo = out_off + (u32)((w * 16 + h) * 8192 + rho * 16);
#pragma unroll
  for (int i = 0; i < 8; ++i) {
    f32x4 v = *(LAS f32x4*)(rp + 2 * i * PITCH) + *(LAS f32x4*)(rp + 2 * i * PITCH + HALF);
    if (!ISA) v += dv * (f32x4){__uint_as_float(xv[i][0] << 16), __uint_as_float(xv[i][0] & 0xffff0000u), __uint_as_float(xv[i][1] << 16), __uint_as_float(xv[i][1] & 0xffff0000u)};
    __builtin_amdgcn_raw_buffer_store_b128(__builtin_bit_cast(u32x4, v), outr, vo, i * 16384, OUT_AUX);
  }
}

#define STAMP(i)
#define STAMP0(i)

#define OUTST 2
DI void out_store(float* p, float v) {
  __hip_atomic_store((GAS float*)p, v, RLX_AGENT);
}

template <int AUX = GATHER_AUX> DI void kv_load8(u32x4 (&v)[8], __amdgpu_buffer_rsrc_t wsr, u32 vo) {
#pragma unroll
  for (int k = 0; k < 8; ++k) v[k] = __builtin_amdgcn_raw_buffer_load_b128(wsr, vo, k * (u32)KVP_BYTES, AUX);
}
DI void kv_check8(u32x4 (&v)[8], __amdgpu_buffer_rsrc_t wsr, u32 vo, u32 tg) {
  const u64 t0 = __builtin_amdgcn_s_memrealtime();
  for (;;) {
    bool any = false;
#pragma unroll
    for (int k = 0; k < 8; ++k) {
      if (!__all(v[k][1] == tg && v[k][3] == tg)) {
        v[k] = __builtin_amdgcn_raw_buffer_load_b128(wsr, vo, k * (u32)KVP_BYTES, 16);
        any = true;
      }
    }
    if (!any) break;
    if (__builtin_amdgcn_s_memrealtime() - t0 > 500000ull) break;
  }
}
DI u32x2 kv_sum8(const u32x4 (&v)[8]) {
  f32x4 s0 = {0.f, 0.f, 0.f, 0.f};
#pragma unroll
  for (int k = 0; k < 8; ++k)
    s0 += (f32x4){__uint_as_float(v[k][0] << 16), __uint_as_float(v[k][0] & 0xffff0000u),
                  __uint_as_float(v[k][2] << 16), __uint_as_float(v[k][2] & 0xffff0000u)};
  return (u32x2){pk2(s0[0], s0[1]), pk2(s0[2], s0[3])};
}

DI void zero16(f32x16& v) {
#pragma unroll
  for (int i = 0; i < 16; ++i) v[i] = 0.f;
}

constexpr int LDS_WPHI = P1_WOFF + 2 * 12288;
constexpr int LDS_IMG = LDS_WPHI + 4096;
constexpr int LDS_RED = 32768;
constexpr int LDS_KVB = 0, LDS_ZB = 8192;
constexpr int LDS_CONST = 147456;
constexpr int LDS_E = LDS_CONST + 2048;
constexpr int LDS_HIN = LDS_E + 4096;

__global__ __launch_bounds__(768, 3) void fused_kernel(Params P) {
  extern __shared__ __attribute__((aligned(16))) char smem_raw[];
  LAS char* lds = (LAS char*)smem_raw;
  u64 pc;
  asm volatile("s_getpc_b64 %0" : "=s"(pc));
  const int tid = threadIdx.x, lane = tid & 63;
  const int w = __builtin_amdgcn_readfirstlane(tid >> 6);
  const int h = lane >> 5, rho = lane & 31;
  const int bid = blockIdx.x, xcd = bid & 7, idx = bid >> 3;
  const bool isA = idx < 16;
  const int li = idx & 15;
  const int c = 4 * (xcd & 1) + (li & 3), cg = 4 * (xcd >> 1) + (li >> 2);
  const int rb = 4 * (xcd & 3) + (li & 3), nb = 4 * (xcd >> 2) + (li >> 2);
  char* ws = P.ws;
  STAMP0(0);
  const __amdgpu_buffer_rsrc_t wsr = __builtin_amdgcn_make_buffer_rsrc(ws, 0, (int)WS_END, 0x00020000);
  const __amdgpu_buffer_rsrc_t outr = __builtin_amdgcn_make_buffer_rsrc(P.out, 0, 2048 * 2048 * 4, 0x00020000);
  GAS u64* epoch = (GAS u64*)(ws + (isA ? WS_EPOCH_A : WS_EPOCH_B));

  if (__builtin_expect(w >= 8, 1)) {
    const int lw = w - 8;
    const u32 ldsb = (u32)(size_t)lds;
    __builtin_amdgcn_s_setprio(3);
    u64 epoch_val = __hip_atomic_load(epoch, RLX_AGENT);
    if (isA) {
      const int hp = cg;
      p1_load<true>(ldsb, (const char*)(P.Ax + (size_t)c * 256 * 1024), (const char*)(P.wqkv + hp * 64), nullptr, lw, lane, ROTA * (2 * xcd),
                    [&](auto) { dma_s((u32)(lane * 16), (const char*)(P.wphi + hp * 2048) + lw * 1024, ldsb + LDS_WPHI + lw * 1024); }, (u32*)(P.ws + (33u << 20)) + bid * 32);
      asm volatile("" : "+v"(epoch_val));
      const u64 tag = next_tag(epoch_val);
      bar();
      {
        const u32 tg = (u32)tag;
        const u32 kvp0 = (u32)(WS_KVP + (size_t)(hp * 8) * KVP_BYTES);
        const u64 t0 = __builtin_amdgcn_s_memrealtime();
        __builtin_amdgcn_s_setprio(0);
        __builtin_amdgcn_s_sleep(SPEC);
        const u32 u0 = 512 + lw * 64 + lane;
        const u32 vo = kvp0 + u0 * 16;
        u32x4 v0[8], v1[8], vz[8];
        kv_load8(v0, wsr, vo); kv_load8(v1, wsr, vo + 4096);
        if (lw == 0) kv_load8(vz, wsr, vo + 8192);
        kv_check8(v0, wsr, vo, tg);
        *(LAS u32x2*)(lds + LDS_KVB + u0 * 8) = kv_sum8(v0);
        kv_check8(v1, wsr, vo + 4096, tg);
        *(LAS u32x2*)(lds + LDS_KVB + (u0 + 256) * 8) = kv_sum8(v1);
        if (lw == 0) {
          kv_check8(vz, wsr, vo + 8192, tg);
          float a = 0.f, b = 0.f;
#pragma unroll
          for (int k = 0; k < 8; ++k) { a += __uint_as_float(vz[k][0]); b += __uint_as_float(vz[k][2]); }
          const u32 hi = pk2(a, b);
          const u32 lo = pk2(a - __uint_as_float(hi << 16), b - __uint_as_float(hi & 0xffff0000u));
          const int f = 2 * (lane & 15), i4 = (lane >> 4) * 2 + (f >> 4);
          LAS char* zp = lds + LDS_ZB + i4 * 64 + ((f >> 2) & 1) * 16 + (((f >> 3) & 1) * 4 + (f & 3)) * 2;
          *(LAS u32*)zp = hi; *(LAS u32*)(zp + 32) = lo;
        }
        wait_lgkm0();
        if (lw == 0) STAMP(11);
      }
      __builtin_amdgcn_s_setprio(3);
      bar(); bar();
      if (lw == 0 && lane == 0) __hip_atomic_store((GAS u64*)(ws + WS_OFLAG + c * 128 + hp * 8), tag, RLX_AGENT);
      p2_load<true>(ldsb, ws + WS_O + (size_t)rb * 4 * 16 * 4096, nullptr, (const char*)(P.wo + nb * 128), lw, lane, ROT2 * (2 * xcd),
                    ws + WS_OFLAG + (rb >> 1) * 128, tag, (rb == 0 && nb == 0 && lw == 0) ? epoch : nullptr);
    } else {
      const int pg = cg;
      p1_load<false>(ldsb, (const char*)(P.Bx + (size_t)c * 256 * 1024), (const char*)(P.bre + (size_t)pg * 64 * 1024),
                     (const char*)(P.bim + (size_t)pg * 64 * 1024), lw, lane, ROTB * (2 * xcd + 1), [](auto) {}, (u32*)(P.ws + (33u << 20)) + bid * 32);
      asm volatile("" : "+v"(epoch_val));
      const u64 tag = next_tag(epoch_val);
      bar();
      if (lw == 0) {
        const f32x2 k1 = *(LAS f32x2*)(lds + LDS_CONST + lane * 32 + 16);
        const float pr = k1[0], pi = k1[1];
        const u32 tg = (u32)tag;
        GAS u64* agg = (GAS u64*)(ws + WS_AGG) + (size_t)pg * 8 * 128;
        LAS f32x2* E = (LAS f32x2*)(lds + LDS_E);
        float ar = 0.f, ai = 0.f;
#pragma unroll
        for (int k = 0; k < 8; ++k) {
          const f32x2 e = E[k * 64 + lane];
          const float a = fmaf(pr, ar, fmaf(-pi, ai, e[0])), b = fmaf(pr, ai, fmaf(pi, ar, e[1]));
          ar = a; ai = b;
        }
        __hip_atomic_store(agg + c * 128 + lane, ((u64)tg << 32) | __float_as_uint(ar), RLX_AGENT);
        __hip_atomic_store(agg + c * 128 + 64 + lane, ((u64)tg << 32) | __float_as_uint(ai), RLX_AGENT);
        float br = pr, bi = pi;
#pragma unroll
        for (int i = 0; i < 3; ++i) { const float a = br * br - bi * bi, b = 2.0f * br * bi; br = a; bi = b; }
        float vr[7], vi[7];
        const u64 t0 = __builtin_amdgcn_s_memrealtime();
        for (;;) {
          u64 x[7], y[7];
#pragma unroll
          for (int k = 0; k < 7; ++k) {
            const int kk = k < c ? k : c;
            x[k] = __hip_atomic_load(agg + kk * 128 + lane, RLX_AGENT);
            y[k] = __hip_atomic_load(agg + kk * 128 + 64 + lane, RLX_AGENT);
          }
          u32 bad = 0;
#pragma unroll
          for (int k = 0; k < 7; ++k) {
            vr[k] = __uint_as_float((u32)x[k]); vi[k] = __uint_as_float((u32)y[k]);
            bad |= k < c ? (((u32)(x[k] >> 32) ^ tg) | ((u32)(y[k] >> 32) ^ tg)) : 0u;
          }
          if (__all(bad == 0)) break;
          if (__builtin_amdgcn_s_memrealtime() - t0 > 500000ull) break;
          __builtin_amdgcn_s_sleep(1);
        }
        float hr = 0.f, hi = 0.f;
#pragma unroll
        for (int k = 0; k < 7; ++k) {
          const float a = fmaf(br, hr, fmaf(-bi, hi, vr[k])), b = fmaf(br, hi, fmaf(bi, hr, vi[k]));
          if (k < c) { hr = a; hi = b; }
        }
        *(LAS f32x2*)(lds + LDS_HIN + lane * 8) = (f32x2){hr, hi};
        wait_lgkm0();
      }
      bar(); bar();
      if (lw == 0 && lane == 0) __hip_atomic_store((GAS u64*)(ws + WS_HFLAG + c * 128 + pg * 8), tag, RLX_AGENT);
      p2_load<false>(ldsb, (const char*)(P.cre + (size_t)nb * 128 * 1024), (const char*)(P.cim + (size_t)nb * 128 * 1024),
                     ws + WS_H + (size_t)rb * 2 * 64 * 4096, lw, lane, ROT2 * (4 * xcd),
                     ws + WS_HFLAG + (rb >> 1) * 128, tag, (rb == 15 && nb == 0 && lw == 0) ? epoch : nullptr);
    }
    return;
  }

  const char* pfqkv = (const char*)P.wqkv + (size_t)(idx * 8 + xcd) * 24576;
  const char* pfcode = (const char*)pc + (u32)(idx & 7) * (CODE_PF_LINES * 128u);
  auto pf_code = [&]() {
    if (w == 1) prefetch_lines((u32)min(lane, (int)CODE_PF_LINES - 1) << 7, pfcode, (u32)(size_t)(lds + LDS_DUMP));
  };
  pf_code();
  if (isA) {
    const int wm = w >> 1, hh = w & 1, hp = cg;
    u32 tg;
    bf16x8 phiq[2][4];
    {
      f32x16 acc[2][3];
#pragma unroll
      for (int m = 0; m < 2; ++m)
#pragma unroll
        for (int j = 0; j < 3; ++j) zero16(acc[m][j]);
      const int gb = xcd * 16 + li;
      const u32 dump = (u32)(size_t)(lds + LDS_DUMP);
      const u32 pfq = (u32)min(lane, w == 3 ? 23 : 15) << (w == 2 ? 11 : 7);
      const u32 pfl = (u32)(lane * 128);
      const char* pfx = (const char*)P.Ax + (size_t)gb * (16 * 2048);
      const char* pfw = (const char*)P.wqkv + (size_t)gb * 3072;
      const char* pfo = (const char*)P.wo + (size_t)gb * 16384;
      auto pf_tile = [&](int T) {
        if (w == 2) prefetch_lines(pfq, pfx + T * 128, dump);
        if (w == 3) prefetch_lines(pfq, pfw + (size_t)T * (64 * 6144), dump);
      };
#pragma unroll
      for (int T = 3; T < PFD; ++T) pf_tile(T);
      tg = __builtin_amdgcn_readfirstlane((u32)next_tag(__hip_atomic_load(epoch, RLX_AGENT)));
      p1_mma<true>(lds, acc, w, lane, [&]() { STAMP0(1); },
                   [&](auto T_) {
                     constexpr int T = decltype(T_)::value;
                     if constexpr (PFIN == 0 && T + PFD < 16) pf_tile(T + PFD);
                     if constexpr (T == PFC) pf_code();
                     if constexpr (WPF == 2 && T < 3) { if (w == 5) prefetch_lines(pfl, pfqkv + T * 8192, dump); }
                     if constexpr (T >= PFT && T < PFT + 8) { if (w == 4) prefetch_lines(pfq, pfo + (T - PFT) * 2048, dump); }
                   });
      STAMP0(2);
      const int blk = (lane >> 4) & 1, q = (lane & 15) >> 2, p = lane & 3;
      bf16x8 wph[2];
#pragma unroll
      for (int t = 0; t < 2; ++t)
        wph[t] = lds_frag_tr(lds + LDS_WPHI + hh * 2048 + (16 * t + 4 * h + q) * 64 + (16 * blk + 4 * p) * 2, 8 * 64);
      f32x16 kv[2];
      zero16(kv[0]); zero16(kv[1]);
      float z[2] = {0.f, 0.f};
      LAS char* img = lds + LDS_IMG + w * 4352;
      const u32 ones = 0x3f803f80u;
#pragma unroll
      for (int m = 0; m < 2; ++m) {
        f32x16 pp;
        u32 ep[8], em[8];
        zero16(pp);
        pp = MFMA(wph[0], pack8(acc[m][1], 0), pp);
        pp = MFMA(wph[1], pack8(acc[m][1], 1), pp);
        dual_softmax(pp, ep, em);
#pragma unroll
        for (int g = 0; g < 4; ++g) {
          const u32x2 a = {ep[2 * g], ep[2 * g + 1]}, b = {em[2 * g], em[2 * g + 1]};
          *(LAS u32x2*)(img + rho * 136 + (8 * g + 4 * h) * 2) = a;
          *(LAS u32x2*)(img + rho * 136 + (32 + 8 * g + 4 * h) * 2) = b;
        }
#pragma unroll
        for (int t = 0; t < 2; ++t) {
          const bf16x8 vf = pack8(acc[m][2], t);
#pragma unroll
          for (int F = 0; F < 2; ++F) {
            const bf16x8 af = lds_frag_tr(img + (16 * t + 4 * h + q) * 136 + (32 * F + 16 * blk + 4 * p) * 2, 8 * 136);
            kv[F] = MFMA(af, vf, kv[F]);
            const u32x4 au = __builtin_bit_cast(u32x4, af);
            asm("v_dot2c_f32_bf16 %0, %1, %5\n\tv_dot2c_f32_bf16 %0, %2, %5\n\tv_dot2c_f32_bf16 %0, %3, %5\n\tv_dot2c_f32_bf16 %0, %4, %5\n\ts_nop 2"
                : "+v"(z[F]) : "v"(au[0]), "v"(au[1]), "v"(au[2]), "v"(au[3]), "v"(ones));
          }
        }
      }
      STAMP0(12);
      {
        LAS char* red = lds + LDS_RED + w * 8192;
#pragma unroll
        for (int F = 0; F < 2; ++F) {
#pragma unroll
          for (int g = 0; g < 4; ++g) {
            f32x4 v = {kv[F][4 * g], kv[F][4 * g + 1], kv[F][4 * g + 2], kv[F][4 * g + 3]};
            *(LAS f32x4*)(red + ((F * 4 + g) * 64 + lane) * 16) = v;
          }
          const float zt = xhalf_sum(z[F]);
          if (h == 0) *(LAS float*)(img + (F * 32 + rho) * 4) = zt;
        }
      }
      wait_lgkm0();
      bar();
      STAMP0(13);
      const u32 kvp_me = (u32)(WS_KVP + (size_t)(hp * 8 + c) * KVP_BYTES);
#pragma unroll
      for (int e = 0; e < 2; ++e) {
        f32x4 s = *(LAS f32x4*)(lds + LDS_RED + e * 8192 + tid * 16);
#pragma unroll
        for (int k = 1; k < 4; ++k) s += *(LAS f32x4*)(lds + LDS_RED + (2 * k + e) * 8192 + tid * 16);
        const u32x4 g = {pk2(s[0], s[1]), tg, pk2(s[2], s[3]), tg};
        __builtin_amdgcn_raw_buffer_store_b128(g, wsr, kvp_me + (e * 512 + tid) * 16, 0, 16);
      }
      if (w == 7) {
        f32x2 s = *(LAS f32x2*)(lds + LDS_IMG + h * 4352 + rho * 8);
#pragma unroll
        for (int k = 1; k < 4; ++k) s += *(LAS f32x2*)(lds + LDS_IMG + (2 * k + h) * 4352 + rho * 8);
        const u32x4 g = {__float_as_uint(s[0]), tg, __float_as_uint(s[1]), tg};
        __builtin_amdgcn_raw_buffer_store_b128(g, wsr, kvp_me + (1024 + lane) * 16, 0, 16);
      }
      STAMP0(3);
#pragma unroll
      for (int m = 0; m < 2; ++m) {
        f32x16 pp;
        u32 ep[8], em[8];
        zero16(pp);
        pp = MFMA(wph[0], pack8(acc[m][0], 0), pp);
        pp = MFMA(wph[1], pack8(acc[m][0], 1), pp);
        dual_softmax(pp, ep, em);
        phiq[m][0] = frag4(ep[0], ep[1], ep[2], ep[3]); phiq[m][1] = frag4(ep[4], ep[5], ep[6], ep[7]);
        phiq[m][2] = frag4(em[0], em[1], em[2], em[3]); phiq[m][3] = frag4(em[4], em[5], em[6], em[7]);
      }
    }
    STAMP0(4);
    {
      const u32 kvp0 = (u32)(WS_KVP + (size_t)(hp * 8) * KVP_BYTES);
      const u64 t0 = __builtin_amdgcn_s_memrealtime();
      u32x4 v[8];
      kv_load8(v, wsr, kvp0 + tid * 16);
      kv_check8(v, wsr, kvp0 + tid * 16, tg);
      *(LAS u32x2*)(lds + LDS_KVB + tid * 8) = kv_sum8(v);
      wait_lgkm0();
    }
    LAS char* kvb = lds + LDS_KVB;
    LAS char* zb = lds + LDS_ZB;
    bar();
    STAMP0(9);
    {
      bf16x8 kvf[4], zf[4];
#pragma unroll
      for (int i = 0; i < 4; ++i) {
        const u32x2 a = *(LAS u32x2*)(kvb + hh * 4096 + ((2 * i) * 64 + lane) * 8);
        const u32x2 b = *(LAS u32x2*)(kvb + hh * 4096 + ((2 * i + 1) * 64 + lane) * 8);
        kvf[i] = frag4(a[0], a[1], b[0], b[1]);
        zf[i] = lds_frag(zb + (hh * 4 + i) * 64 + (rho >> 4) * 32 + h * 16);
      }
#pragma unroll
      for (int m = 0; m < 2; ++m) {
        f32x16 ot, dn;
        zero16(ot); zero16(dn);
#pragma unroll
        for (int i = 0; i < 4; ++i) { dn = MFMA(zf[i], phiq[m][i], dn); ot = MFMA(kvf[i], phiq[m][i], ot); }
        const float r = __builtin_amdgcn_rcpf((dn[0] + dn[8]) + 1e-6f);
        u32 d[8];
#pragma unroll
        for (int i = 0; i < 8; ++i) d[i] = pk2(ot[2 * i] * r, ot[2 * i + 1] * r);
        const u32 obase = (u32)(WS_O + (size_t)((c * 8 + wm * 2 + m) * 16 + hp) * 4096);
#pragma unroll
        for (int i = 0; i < 2; ++i) {
          auto r0 = __builtin_amdgcn_permlane32_swap(d[4 * i], d[4 * i + 2], false, false);
          auto r1 = __builtin_amdgcn_permlane32_swap(d[4 * i + 1], d[4 * i + 3], false, false);
          u32x4 v = {r0[0], r1[0], r0[1], r1[1]};
          __builtin_amdgcn_raw_buffer_store_b128(v, wsr, obase + ((4 * hh + 2 * i + h) * 32 + rho) * 16, 0, 16);
        }
      }
    }
    wait_vm<0>();
    wait_lgkm0();
    bar();
    STAMP0(5);
    f32x16 acc[2][2];
    zero16(acc[0][0]); zero16(acc[0][1]); zero16(acc[1][0]); zero16(acc[1][1]);
    p2_mma<true>(lds, acc, w, lane, [&]() { STAMP0(6); });
    STAMP0(7);
    u32x2 nox[8] = {};
    p2_store<true>(lds, acc, w, lane, outr, (u32)(rb * 128 * 8192 + nb * 512), nox, nox[0]);
    STAMP0(8);
  } else {
    const int wm = w >> 1, pn = w & 1, pg = cg, pl = pn * 32 + rho;
    u32x2 xv[8], dsk;
    {
      f32x16 acc[2][2];
      zero16(acc[0][0]); zero16(acc[0][1]); zero16(acc[1][0]); zero16(acc[1][1]);
      const int gb = xcd * 16 + li;
      const u32 dump = (u32)(size_t)(lds + LDS_DUMP);
      const u32 pfq = (u32)min(lane, w == 4 ? 31 : 15) << (w == 4 ? 7 : 11);
      const u32 pfl = (u32)(lane * 128);
      const char* pfx = (const char*)P.Bx + (size_t)gb * (16 * 2048);
      const char* pfw = (const char*)((gb & 64) ? P.bim : P.bre) + (size_t)(gb & 63) * (16 * 2048);
      const char* pfo = (const char*)((gb & 64) ? P.cim : P.cre) + (size_t)(gb & 63) * 32768;
      auto pf_tile = [&](int T) {
        if (w == 2) prefetch_lines(pfq, pfx + T * 128, dump);
        if (w == 3) prefetch_lines(pfq, pfw + T * 128, dump);
      };
#pragma unroll
      for (int T = 3; T < PFD; ++T) pf_tile(T);
      if (w == 7) {
        const int pidx = pg * 64 + lane;
        const float la = bf2f(P.lre[pidx]), lb = bf2f(P.lim[pidx]), ldt = bf2f(P.logdt[pidx]);
        const float dt = bf2f((u16)(pk2(expf(ldt), 0.f) & 0xffffu));
        const float er = expf(dt * la);
        float sn, cs;
        sincosf(dt * lb, &sn, &cs);
        const float lr = er * cs, li_ = er * sn;
        const float nr = lr - 1.0f, dn = 1.0f / (la * la + lb * lb);
        const float c2r = 2.0f * (nr * la + li_ * lb) * dn, c2i = 2.0f * (li_ * la - nr * lb) * dn;
        float pr = lr, pi = li_;
#pragma unroll
        for (int i = 0; i < 5; ++i) { const float a = pr * pr - pi * pi, b = 2.0f * pr * pi; pr = a; pi = b; }
        *(LAS f32x4*)(lds + LDS_CONST + lane * 32) = (f32x4){lr, li_, c2r, c2i};
        *(LAS f32x2*)(lds + LDS_CONST + lane * 32 + 16) = (f32x2){pr, pi};
        wait_lgkm0();
      }
      p1_mma<false>(lds, acc, w, lane, [&]() { STAMP0(1); },
                    [&](auto T_) {
                      constexpr int T = decltype(T_)::value;
                      if constexpr (PFINB == 0 && T + PFD < 16) pf_tile(T + PFD);
                      if constexpr (T == PFC) pf_code();
                      if constexpr (WPF == 2 && T < 3) { if (w == 5) prefetch_lines(pfl, pfqkv + T * 8192, dump); }
                      if constexpr (T >= PFT && T < PFT + 8) { if (w == 4) prefetch_lines(pfq, pfo + (T - PFT) * 4096, dump); }
                    });
      STAMP0(2);
      const f32x4 k0 = *(LAS f32x4*)(lds + LDS_CONST + pl * 32);
      const f32x2 k1 = *(LAS f32x2*)(lds + LDS_CONST + pl * 32 + 16);
      const float lr = k0[0], li_ = k0[1], c2r = k0[2], c2i = k0[3], pr = k1[0], pi = k1[1];
      float gr = 0.f, gi = 0.f;
#pragma unroll
      for (int m = 0; m < 2; ++m)
#pragma unroll
        for (int r = 0; r < 16; ++r) {
          const float a = fmaf(lr, gr, fmaf(-li_, gi, acc[m][0][r]));
          const float b = fmaf(lr, gi, fmaf(li_, gr, acc[m][1][r]));
          gr = a; gi = b;
          acc[m][0][r] = a; acc[m][1][r] = b;
        }
      const int sg = 2 * wm + h;
      LAS f32x2* E = (LAS f32x2*)(lds + LDS_E);
      E[sg * 64 + pl] = (f32x2){gr, gi};
      wait_lgkm0();
      bar();
      STAMP0(3);
      float qr = 0.f, qi = 0.f, wr_ = 1.f, wi_ = 0.f;
#pragma unroll
      for (int k = 0; k < 7; ++k) {
        const f32x2 e = E[k * 64 + pl];
        const float a = fmaf(pr, qr, fmaf(-pi, qi, e[0])), b = fmaf(pr, qi, fmaf(pi, qr, e[1]));
        const float a2 = wr_ * pr - wi_ * pi, b2 = wr_ * pi + wi_ * pr;
        const bool take = k < sg;
        qr = take ? a : qr; qi = take ? b : qi; wr_ = take ? a2 : wr_; wi_ = take ? b2 : wi_;
      }
      f32x2 hl[32], pw[32];
      {
        const f32x2 ca = {c2r, -c2r}, cb = {-c2i, -c2i};
        f32x2 pcur = {lr, -li_};
#pragma unroll
        for (int t = 0; t < 32; ++t) {
          const f32x2 g = {acc[t >> 4][0][t & 15], acc[t >> 4][1][t & 15]};
          hl[t] = __builtin_elementwise_fma(cb, (f32x2){g[1], g[0]}, ca * g);
          pw[t] = pcur;
          pcur = __builtin_elementwise_fma((f32x2){li_, -li_}, (f32x2){pcur[1], pcur[0]}, pcur * lr);
        }
#pragma unroll
        for (int t = 0; t < 32; ++t) asm volatile("" : "+v"(hl[t]), "+v"(pw[t]));
        asm volatile("" : "+v"(qr), "+v"(qi), "+v"(wr_), "+v"(wi_));
      }
      bar();
      STAMP0(4);
      float cwr, cwi;
      {
        const f32x2 hin = *(LAS f32x2*)(lds + LDS_HIN + pl * 8);
        gr = fmaf(wr_, hin[0], fmaf(-wi_, hin[1], qr));
        gi = fmaf(wr_, hin[1], fmaf(wi_, hin[0], qi));
        cwr = c2r * gr - c2i * gi; cwi = c2r * gi + c2i * gr;
      }
      const u32 hbase = (u32)(WS_H + (size_t)((((c * 4 + wm) * 16 + pg) * 2) * 2 + pn) * 4096) + (u32)(((rho >> 2) * 8 + 4 * h) * 64 + (rho & 3) * 16);
#pragma unroll
      for (int tc = 0; tc < 4; ++tc) {
        f32x2 o[8];
#pragma unroll
        for (int r = 0; r < 8; ++r) {
          const f32x2 pv = pw[8 * tc + r];
          o[r] = __builtin_elementwise_fma((f32x2){cwi, -cwi}, (f32x2){pv[1], pv[0]}, __builtin_elementwise_fma((f32x2){cwr, cwr}, pv, hl[8 * tc + r]));
        }
        const u32x4 vr = {pk2(o[0][0], o[1][0]), pk2(o[2][0], o[3][0]), pk2(o[4][0], o[5][0]), pk2(o[6][0], o[7][0])};
        const u32x4 vi = {pk2(o[0][1], o[1][1]), pk2(o[2][1], o[3][1]), pk2(o[4][1], o[5][1]), pk2(o[6][1], o[7][1])};
        __builtin_amdgcn_raw_buffer_store_b128(vr, wsr, hbase + tc * 64, 0, 16);
        __builtin_amdgcn_raw_buffer_store_b128(vi, wsr, hbase + tc * 64 + 8192, 0, 16);
      }
    }
    wait_vm<0>();
    wait_lgkm0();
    bar();
    STAMP0(5);
      {
        const u16* xp = P.Bx + (size_t)(rb * 128 + w * 16 + h) * 1024 + nb * 128 + rho * 4;
#pragma unroll
        for (int i = 0; i < 8; ++i) xv[i] = *(const u32x2*)(xp + 2 * i * 1024);
        dsk = *(const u32x2*)(P.dskip + nb * 128 + rho * 4);
      }
    f32x16 acc[2][2];
    zero16(acc[0][0]); zero16(acc[0][1]); zero16(acc[1][0]); zero16(acc[1][1]);
    p2_mma<false>(lds, acc, w, lane, [&]() { STAMP0(6); });
    STAMP0(7);
    p2_store<false>(lds, acc, w, lane, outr, (u32)(rb * 128 * 8192 + 4096 + nb * 512), xv, dsk);
    STAMP0(8);
  }
}

extern "C" void kernel_launch(void* const* d_in, const int* in_sizes, int n_in,
                              void* d_out, int out_size, void* d_ws, size_t ws_size,
                              hipStream_t stream) {
  Params P;
  P.Ax = (const u16*)d_in[0]; P.wqkv = (const u16*)d_in[1]; P.wphi = (const u16*)d_in[2]; P.wo = (const u16*)d_in[3];
  P.Bx = (const u16*)d_in[4]; P.lre = (const u16*)d_in[5]; P.lim = (const u16*)d_in[6];
  P.bre = (const u16*)d_in[7]; P.bim = (const u16*)d_in[8]; P.cre = (const u16*)d_in[9]; P.cim = (const u16*)d_in[10];
  P.logdt = (const u16*)d_in[11]; P.dskip = (const u16*)d_in[12];
  P.out = (float*)d_out; P.ws = (char*)d_ws;
  (void)hipFuncSetAttribute((const void*)fused_kernel, hipFuncAttributeMaxDynamicSharedMemorySize, LDS_BYTES);
  fused_kernel<<<256, 768, LDS_BYTES, stream>>>(P);
}
```

```cpp
#include <hip/hip_runtime.h>
#include <hip/hip_bf16.h>

typedef unsigned short u16;
typedef unsigned int u32;
typedef unsigned long long u64;
typedef __bf16 bf16x8 __attribute__((ext_vector_type(8)));
typedef __bf16 bf16x2 __attribute__((ext_vector_type(2)));
typedef short s16x4 __attribute__((ext_vector_type(4)));
typedef short s16x8 __attribute__((ext_vector_type(8)));
typedef float f32x16 __attribute__((ext_vector_type(16)));
typedef float f32x4 __attribute__((ext_vector_type(4)));
typedef float f32x2 __attribute__((ext_vector_type(2)));
typedef u32 u32x4 __attribute__((ext_vector_type(4)));
typedef u32 u32x2 __attribute__((ext_vector_type(2)));
#define LAS __attribute__((address_space(3)))
#define GAS __attribute__((address_space(1)))
#define DI __device__ __forceinline__
#define RLX_AGENT __ATOMIC_RELAXED, __HIP_MEMORY_SCOPE_AGENT

constexpr size_t WS_EPOCH_A = 0;
constexpr size_t WS_EPOCH_B = 256;
constexpr size_t WS_KVFLAG = 4096;
constexpr size_t WS_OFLAG = WS_KVFLAG + 16 * 128;
constexpr size_t WS_HFLAG = WS_OFLAG + 8 * 128;
constexpr size_t WS_AGG = 65536;
constexpr size_t WS_KVP = WS_AGG + 16 * 8 * 1024;
constexpr size_t KVP_BYTES = (1024 + 64) * 16;
constexpr size_t WS_O = 4u << 20;
constexpr size_t WS_H = 8u << 20;
constexpr size_t WS_END = 16u << 20;

constexpr int LDS_BYTES = 163840;

struct Params {
  const u16* Ax; const u16* wqkv; const u16* wphi; const u16* wo;
  const u16* Bx; const u16* lre; const u16* lim; const u16* bre; const u16* bim;
  const u16* cre; const u16* cim; const u16* logdt; const u16* dskip;
  float* out; char* ws;
};

DI float bf2f(u16 v) { return __uint_as_float(((u32)v) << 16); }
DI u32 pk2(float a, float b) {
  f32x2 v = {a, b};
  bf16x2 r = __builtin_convertvector(v, bf16x2);
  return __builtin_bit_cast(u32, r);
}
DI bf16x8 pack8(const f32x16& x, int s) {
  u32x4 p;
  p[0] = pk2(x[8 * s + 0], x[8 * s + 1]); p[1] = pk2(x[8 * s + 2], x[8 * s + 3]);
  p[2] = pk2(x[8 * s + 4], x[8 * s + 5]); p[3] = pk2(x[8 * s + 6], x[8 * s + 7]);
  return __builtin_bit_cast(bf16x8, p);
}
#define MFMA(a, b, c) __builtin_amdgcn_mfma_f32_32x32x16_bf16((a), (b), (c), 0, 0, 0)
#define LMFMA MFMA
DI bf16x8 lds_frag(LAS char* p) { return *(LAS bf16x8*)p; }
DI s16x4 lds_tr(LAS char* __restrict__ p) { return __builtin_amdgcn_ds_read_tr16_b64_v4i16((LAS s16x4*)p); }
DI bf16x8 lds_frag_tr(LAS char* p, int off2) {
  s16x4 lo = lds_tr(p), hi = lds_tr(p + off2);
  s16x8 v = __builtin_shufflevector(lo, hi, 0, 1, 2, 3, 4, 5, 6, 7);
  return __builtin_bit_cast(bf16x8, v);
}
template <int OFF = 0> DI void dma_s(u32 voff, const char* sbase, u32 ldsaddr) {
  asm volatile("s_mov_b32 m0, %2\n\ts_nop 0\n\tglobal_load_lds_dwordx4 %0, %1 offset:%3" ::"v"(voff), "s"(sbase), "s"(ldsaddr), "n"(OFF) : "memory");
}
DI void dma_s4_dev(u32 voff, const char* sbase, u32 ldsaddr) {
  asm volatile("s_mov_b32 m0, %2\n\ts_nop 0\n\tglobal_load_lds_dwordx4 %0, %1 sc1\n\tglobal_load_lds_dwordx4 %0, %1 offset:1024 sc1\n\t"
               "global_load_lds_dwordx4 %0, %1 offset:2048 sc1\n\tglobal_load_lds_dwordx4 %0, %1 offset:3072 sc1"
               ::"v"(voff), "s"(sbase), "s"(ldsaddr) : "memory");
}
template <int N> DI void wait_vm() { asm volatile("s_waitcnt vmcnt(%0)" ::"n"(N) : "memory"); }
DI void wait_lgkm0() { asm volatile("" ::: "memory"); __builtin_amdgcn_s_waitcnt(0xC07F); asm volatile("" ::: "memory"); }
#define SB() __builtin_amdgcn_sched_barrier(0)
DI void bar() { asm volatile("" ::: "memory"); __builtin_amdgcn_s_barrier(); asm volatile("" ::: "memory"); }
DI float xhalf_max(float v) {
  auto r = __builtin_amdgcn_permlane32_swap(__float_as_uint(v), __float_as_uint(v), false, false);
  return fmaxf(__uint_as_float(r[0]), __uint_as_float(r[1]));
}
DI float xhalf_min(float v) {
  auto r = __builtin_amdgcn_permlane32_swap(__float_as_uint(v), __float_as_uint(v), false, false);
  return fminf(__uint_as_float(r[0]), __uint_as_float(r[1]));
}
DI float xhalf_sum(float v) {
  auto r = __builtin_amdgcn_permlane32_swap(__float_as_uint(v), __float_as_uint(v), false, false);
  return __uint_as_float(r[0]) + __uint_as_float(r[1]);
}
DI u64 next_tag(u64 e) { return e * 6364136223846793005ull + 1442695040888963407ull; }

DI void wait_flags(const char* flags, int n, u64 tag, int lane) {
  const GAS u64* f = (const GAS u64*)flags + (lane < n ? lane : 0);
  const u64 t0 = __builtin_amdgcn_s_memrealtime();
  for (;;) {
    u64 v = __hip_atomic_load(f, RLX_AGENT);
    if (__all(v == tag)) break;
    if (__builtin_amdgcn_s_memrealtime() - t0 > 500000ull) break;
    __builtin_amdgcn_s_sleep(1);
  }
}

DI void dual_softmax(const f32x16& p, f32x16& ep, f32x16& em) {
  float mx = p[0], mn = p[0];
#pragma unroll
  for (int i = 1; i < 16; ++i) { mx = fmaxf(mx, p[i]); mn = fminf(mn, p[i]); }
  mx = xhalf_max(mx); mn = xhalf_min(mn);
  const float L2E = 1.4426950408889634f;
  const float bp = -mx * L2E, bm = mn * L2E;
  float sp = 0.f, sm = 0.f;
#pragma unroll
  for (int i = 0; i < 16; ++i) {
    ep[i] = __builtin_amdgcn_exp2f(fmaf(p[i], L2E, bp));
    em[i] = __builtin_amdgcn_exp2f(fmaf(p[i], -L2E, bm));
    sp += ep[i]; sm += em[i];
  }
  sp = xhalf_sum(sp); sm = xhalf_sum(sm);
  const float rp = __builtin_amdgcn_rcpf(sp), rm = __builtin_amdgcn_rcpf(sm);
#pragma unroll
  for (int i = 0; i < 16; ++i) { ep[i] *= rp; em[i] *= rm; }
}

#define FRONT -1
template <int NM, int ND> DI void interleave() {
  constexpr int FR = FRONT > 0 ? FRONT : NM + FRONT;
  constexpr int DPM = (ND + FR - 1) / FR;
#pragma unroll
  for (int i = 0; i < NM; ++i) {
    __builtin_amdgcn_sched_group_barrier(0x008, 1, 0);
    if (i * DPM < ND) __builtin_amdgcn_sched_group_barrier(0x100, DPM, 0);
  }
}
#define IC(n) std::integral_constant<int, (n)>{}
constexpr int LDS_DUMP = 163584;
DI void prefetch_lines(u32 voff, const char* sbase, u32 dump) {
  asm volatile("s_mov_b32 m0, %2\n\ts_nop 0\n\tglobal_load_lds_dword %0, %1" ::"v"(voff), "s"(sbase), "s"(dump) : "memory");
}

constexpr int P1_WOFF = 98304;
#define HINT_SLEEP 4
#define GATHER_AUX 0
constexpr u32 CODE_PF_BYTES = 6 * 8192;
#define PFT 6
#define PFIN 0
#define PFD 5
#define ROTA 0
#define ROTB 0
#define WPF 0
#define PFINB 0
#define ROT2 1

template <int N, class F, int I = 0> DI void static_for(F&& f) {
  if constexpr (I < N) { f(std::integral_constant<int, I>{}); static_for<N, F, I + 1>(static_cast<F&&>(f)); }
}

template <bool ISA, class HOOK>
DI void p1_load(u32 ldsb, const char* xg, const char* wg0, const char* wg1, int lw, int lane, int rot, HOOK&& hook, u32* ttout = nullptr) {
  constexpr int WSLOT = ISA ? 12288 : 16384, NW = ISA ? 5 : 3;
  const u32 offE = (u32)((lane >> 3) * 2048 + (((lane & 7) ^ (lane >> 4)) * 16));
  const u32 offO = (u32)((8 + (lane >> 3)) * 2048 + (((lane & 7) ^ (4 + (lane >> 4))) * 16));
  const u32 offK = (u32)((lane >> 3) * 6144 + (((lane & 7) ^ (((lane >> 4) & 1) << 2)) * 16));
  const char* xb = xg + lw * (64 * 2048);
  const char* wb = ISA ? wg0 + lw * (8 * 6144) : (lw < 2 ? wg0 : wg1) + (lw & 1) * (32 * 2048);
  auto issue_x = [&](int T, int so) {
    const u32 l = ldsb + so + lw * 8192;
    const char* g = xb + ((T + rot) & 15) * 128;
#pragma unroll
    for (int a = 0; a < 4; ++a) { dma_s(offE, g + a * 32768, l + a * 2048); dma_s(offO, g + a * 32768, l + a * 2048 + 1024); }
  };
  auto issue_w = [&](int u, int so) {
    if constexpr (ISA) {
      const u32 l = ldsb + P1_WOFF + so + lw * 1024;
      const char* g = wb + (size_t)((u + 2 * rot) & 31) * (32 * 6144);
      dma_s(offK, g, l); dma_s(offK, g + 2048, l + 4096); dma_s(offK, g + 4096, l + 8192);
    } else {
      const u32 l = ldsb + P1_WOFF + so + lw * 4096;
      const char* g = wb + ((u + rot) & 15) * 128;
      dma_s(offE, g, l); dma_s(offO, g, l + 1024); dma_s(offE, g + 32768, l + 2048); dma_s(offO, g + 32768, l + 3072);
    }
  };
  auto wnext = [&](int o) { return o + WSLOT == NW * WSLOT ? 0 : o + WSLOT; };
  auto xnext = [&](int o) { return o == 65536 ? 0 : o + 32768; };
  int xso = 0, wso = 0;
  if constexpr (ISA) {
    issue_w(0, 0); issue_x(0, 0); issue_w(1, WSLOT);
    wait_vm<3>(); bar();
    issue_w(2, 2 * WSLOT); issue_x(1, 32768); issue_w(3, 3 * WSLOT); issue_w(4, 4 * WSLOT); issue_x(2, 65536);
#pragma unroll 1
    for (int T = 0; T < 13; ++T) {
      const int ws1 = wnext(wso);
      wait_vm<9 + 16>();
      bar();
      issue_w(2 * T + 5, wso);
      wait_vm<9 + 8>(); bar();
      issue_w(2 * T + 6, ws1); issue_x(T + 3, xso);
      wso = wnext(ws1); xso = xnext(xso);
    }
    wait_vm<9 + 16>(); bar();
    issue_w(31, wso);
    wait_vm<9 + 8>(); bar();
    hook(IC(0));
    wait_vm<6 + 8 + 1>(); bar();
    wait_vm<3 + 1>(); bar();
    wait_vm<0>(); bar();
  } else {
    issue_w(0, 0); issue_x(0, 0); issue_w(1, WSLOT); issue_x(1, 32768);
    wait_vm<12>(); bar();
    issue_w(2, 2 * WSLOT); issue_x(2, 65536);
#pragma unroll 1
    for (int T = 0; T < 13; ++T) {
      wait_vm<12>();
      bar();
      issue_w(T + 3, wso); issue_x(T + 3, xso);
      wso = wnext(wso); xso = xnext(xso);
    }
    wait_vm<12>(); bar();
    wait_vm<0>(); bar();
  }
}

template <bool ISA, class ST, class PF>
DI void p1_mma(LAS char* lds, f32x16 (&acc)[2][ISA ? 3 : 2], int w, int lane, ST&& st, PF&& pf) {
  constexpr int NT = ISA ? 3 : 2;
  constexpr int WSLOT = ISA ? 12288 : 16384;
  constexpr int NM = 2 * NT, ND = ISA ? 8 : 4;
  const int wm = w >> 1, wn = w & 1, h = lane >> 5, rho = lane & 31;
  const int arow = wm * 64 + (ISA ? rho : (32 * ((rho >> 2) & 1) + (rho & 3) + 4 * (rho >> 3)));
  constexpr int AM = ISA ? 32 * 128 : 16 * 128;
  const int aoff = arow * 128 + ((h ^ ((arow >> 1) & 7)) * 16);
  int boff;
  if constexpr (ISA) {
    const int blk = (lane >> 4) & 1, q = (lane & 15) >> 2, p = lane & 3;
    const int c = (4 * wn + 2 * blk + (p >> 1)) ^ (((q >> 1) & 1) << 2);
    boff = P1_WOFF + (8 * h + q) * 128 + c * 16 + 8 * (p & 1);
  } else {
    const int brow = wn * 32 + rho;
    boff = P1_WOFF + brow * 128 + ((h ^ ((brow >> 1) & 7)) * 16);
  }
  LAS char* pa[2][4];
  LAS char* pb[4];
#pragma unroll
  for (int s4 = 0; s4 < 4; ++s4) {
    pa[0][s4] = lds + (aoff ^ (s4 * 32)); pa[1][s4] = pa[0][s4] + 65536;
    pb[s4] = lds + (ISA ? boff : (boff ^ (s4 * 32)));
    asm volatile("" : "+v"(pa[0][s4]), "+v"(pa[1][s4]));
    if (!ISA || s4 == 0) asm volatile("" : "+v"(pb[s4]));
  }
  bf16x8 fa0[2], fb0[NT], fa1[2], fb1[NT];
  auto rd = [&](bf16x8 (&fa)[2], bf16x8 (&fb)[NT], auto T_, auto s4_) {
    constexpr int T = decltype(T_)::value, s4 = decltype(s4_)::value;
    constexpr int xs = T % 3;
    LAS char* a = pa[xs >> 1][s4] + (xs & 1) * 32768;
    fa[0] = lds_frag(a); fa[1] = lds_frag(a + AM);
    if constexpr (ISA) {
      LAS char* b = pb[0] + ((2 * T + (s4 >> 1)) % 5) * WSLOT + (s4 & 1) * 2048;
#pragma unroll
      for (int j = 0; j < 3; ++j) fb[j] = lds_frag_tr(b + j * 4096, 512);
    } else {
      LAS char* b = pb[s4] + xs * WSLOT;
      fb[0] = lds_frag(b); fb[1] = lds_frag(b + 64 * 128);
    }
  };
  auto mm = [&](bf16x8 (&fa)[2], bf16x8 (&fb)[NT]) {
#pragma unroll
    for (int m = 0; m < 2; ++m) {
      if constexpr (ISA) {
        acc[m][0] = LMFMA(fb[0], fa[m], acc[m][0]);
        acc[m][1] = LMFMA(fb[1], fa[m], acc[m][1]);
        acc[m][2] = LMFMA(fa[m], fb[2], acc[m][2]);
      } else {
        acc[m][0] = LMFMA(fa[m], fb[0], acc[m][0]);
        acc[m][1] = LMFMA(fa[m], fb[1], acc[m][1]);
      }
    }
  };
  bar();
  st();
  rd(fa0, fb0, IC(0), IC(0));
  static_for<16>([&](auto T_) {
    constexpr int T = decltype(T_)::value;
    rd(fa1, fb1, T_, IC(1)); mm(fa0, fb0); interleave<NM, ND>(); SB();
    if constexpr (ISA) { wait_lgkm0(); bar(); }
    rd(fa0, fb0, T_, IC(2)); mm(fa1, fb1); interleave<NM, ND>(); SB();
    pf(T_);
    rd(fa1, fb1, T_, IC(3)); mm(fa0, fb0); interleave<NM, ND>(); SB();
    if constexpr (T < 15) { wait_lgkm0(); bar(); rd(fa0, fb0, IC(T + 1), IC(0)); }
    mm(fa1, fb1); interleave<NM, T < 15 ? ND : 0>(); SB();
  });
}

template <bool ISA>
DI void p2_load(u32 ldsb, const char* xg0, const char* xg1, const char* yg, int lw, int lane,
                int rot, const char* flags, u64 tag, GAS u64* epoch_to_bump) {
  constexpr int NS = 4, STAGE = 32768, NK = ISA ? 16 : 32;
  const u32 lin = (u32)(lane * 16);
  u32 swE, swO;
  const char *wb0, *wb1, *hb;
  if constexpr (ISA) {
    swE = (u32)((lane >> 3) * 2048 + (((lane & 7) ^ (((lane >> 4) & 1) << 2)) * 16));
    swO = swE + 8 * 2048;
    wb0 = wb1 = yg + (lw & 1) * (32 * 2048) + (lw >> 1) * 128;
    hb = xg0 + lw * (16 * 4096);
  } else {
    swE = (u32)((lane >> 3) * 2048 + (((lane & 7) ^ (lane >> 4)) * 16));
    swO = (u32)((8 + (lane >> 3)) * 2048 + (((lane & 7) ^ (4 + (lane >> 4))) * 16));
    wb0 = xg0 + lw * (32 * 2048); wb1 = xg1 + lw * (32 * 2048);
    hb = yg + (lw >> 1) * (64 * 4096) + (lw & 1) * 4096;
  }
  auto issue_w = [&](int st, int so) {
    st = (st + rot) & (NK - 1);
    const u32 l = ldsb + so + (ISA ? 16384 : 0) + lw * 4096;
    const char* g = ISA ? wb0 + (size_t)st * (64 * 2048) : ((st & 1) ? wb1 : wb0) + (st >> 1) * 128;
    dma_s(swE, g, l); dma_s(swO, g, l + 1024); dma_s(swE, g + 32768, l + 2048); dma_s(swO, g + 32768, l + 3072);
  };
  auto issue_h = [&](int st, int so) {
    st = (st + rot) & (NK - 1);
    dma_s4_dev(lin, hb + st * (ISA ? 4096 : 8192), ldsb + so + (ISA ? 0 : 16384) + lw * 4096);
  };
#pragma unroll
  for (int s = 0; s < NS; ++s) issue_w(s, s * STAGE);
  if (lw == 0) {
    wait_flags(flags, 16, tag, lane);
    if (epoch_to_bump != nullptr && lane == 0) __hip_atomic_store(epoch_to_bump, tag, RLX_AGENT);
  }
  bar();
#pragma unroll
  for (int s = 0; s < NS; ++s) issue_h(s, s * STAGE);
  wait_vm<12>(); bar();
  int so = 0;
  auto nxt = [&]() { so = so == (NS - 1) * STAGE ? 0 : so + STAGE; };
  wait_vm<8>(); bar(); issue_h(NS + 0, so); issue_w(NS + 0, so); nxt();
  wait_vm<12>(); bar(); issue_h(NS + 1, so); issue_w(NS + 1, so); nxt();
#pragma unroll 1
  for (int t = 2; t < NK - NS; ++t) { wait_vm<16>(); bar(); issue_h(t + NS, so); issue_w(t + NS, so); nxt(); }
  wait_vm<16>(); bar(); wait_vm<8>(); bar(); wait_vm<0>(); bar();
  bar(); bar();
}

template <bool ISA, class ST>
DI void p2_mma(LAS char* lds, f32x16 (&acc)[2][2], int w, int lane, ST&& st) {
  constexpr int NS = 4, STAGE = 32768, NK = ISA ? 16 : 32;
  const int kh = w >> 2, wx = (w >> 1) & 1, wy = w & 1, h = lane >> 5, rho = lane & 31;
  const int blk = (lane >> 4) & 1, q = (lane & 15) >> 2, p = lane & 3;
  int xo, yo;
  if constexpr (ISA) {
    xo = wx * 8192 + kh * 2048 + h * 512 + rho * 16;
    yo = 16384 + wy * 8192 + (32 * kh + 8 * h + q) * 128 + (((2 * blk + (p >> 1)) ^ (((q >> 1) & 1) << 2)) * 16) + 8 * (p & 1);
  } else {
    const int xrow = wx * 64 + rho;
    xo = xrow * 128 + (((4 * kh + h) ^ ((xrow >> 1) & 7)) * 16);
    yo = 16384 + wy * 8192 + kh * 4096 + (2 * h * 8 + 2 * blk + (p >> 1)) * 64 + q * 16 + 8 * (p & 1);
  }
  LAS char* px[2][2];
  LAS char* py[2][2];
#pragma unroll
  for (int wdw = 0; wdw < 2; ++wdw) {
    px[wdw][0] = lds + wdw * 65536 + xo; px[wdw][1] = lds + wdw * 65536 + (xo ^ 32);
    py[wdw][0] = lds + wdw * 65536 + yo; py[wdw][1] = lds + wdw * 65536 + (yo ^ 64);
    asm volatile("" : "+v"(px[wdw][0]), "+v"(py[wdw][0]));
    if (ISA) asm volatile("" : "+v"(py[wdw][1])); else asm volatile("" : "+v"(px[wdw][1]));
  }
  bf16x8 fx0[2], fy0[2], fx1[2], fy1[2];
  auto rd = [&](bf16x8 (&fx)[2], bf16x8 (&fy)[2], auto slot_, auto step_) {
    constexpr int slot = decltype(slot_)::value, step = decltype(step_)::value;
    constexpr int wdw = slot >> 1, so = (slot & 1) * STAGE;
    if constexpr (ISA) {
      LAS char* x = px[wdw][0] + so + step * 1024;
      fx[0] = lds_frag(x); fx[1] = lds_frag(x + 4096);
      fy[0] = lds_frag_tr(py[wdw][0] + so + step * 2048, 512);
      fy[1] = lds_frag_tr(py[wdw][1] + so + step * 2048, 512);
    } else {
      LAS char* x = px[wdw][step] + so;
      fx[0] = lds_frag(x); fx[1] = lds_frag(x + 32 * 128);
      fy[0] = lds_frag_tr(py[wdw][0] + so + step * 2048, 512);
      fy[1] = lds_frag_tr(py[wdw][0] + so + step * 2048 + 256, 512);
    }
  };
  auto mm = [&](bf16x8 (&fx)[2], bf16x8 (&fy)[2]) {
#pragma unroll
    for (int i = 0; i < 2; ++i)
#pragma unroll
      for (int j = 0; j < 2; ++j)
        acc[i][j] = ISA ? LMFMA(fx[i], fy[j], acc[i][j]) : LMFMA(fy[j], fx[i], acc[i][j]);
  };
  bar();
  bar();
  st();
  rd(fx0, fy0, IC(0), IC(0));
  auto stage = [&](auto slot_, auto last_) {
    constexpr int slot = decltype(slot_)::value;
    constexpr bool LAST = decltype(last_)::value;
    rd(fx1, fy1, slot_, IC(1)); mm(fx0, fy0); interleave<4, 6>(); SB();
    if constexpr (!LAST) { wait_lgkm0(); bar(); rd(fx0, fy0, IC((slot + 1) % NS), IC(0)); }
    mm(fx1, fy1); interleave<4, LAST ? 0 : 6>(); SB();
  };
#pragma unroll 1
  for (int t = 0; t < NK / NS - 1; ++t) {
    stage(IC(0), std::false_type{}); stage(IC(1), std::false_type{}); stage(IC(2), std::false_type{}); stage(IC(3), std::false_type{});
  }
  stage(IC(0), std::false_type{}); stage(IC(1), std::false_type{}); stage(IC(2), std::false_type{}); stage(IC(3), std::true_type{});
}

DI void p2_combine(LAS char* lds, f32x16 (&acc)[2][2], int w, int lane) {
  const int kh = w >> 2;
  bar();
  LAS char* mine = lds + w * 8192 + lane * 16;
  LAS char* theirs = lds + (w ^ 4) * 8192 + lane * 16;
#pragma unroll
  for (int j = 0; j < 2; ++j)
#pragma unroll
    for (int g = 0; g < 4; ++g) {
      const f32x16& src = kh ? acc[0][j] : acc[1][j];
      f32x4 v = {src[4 * g], src[4 * g + 1], src[4 * g + 2], src[4 * g + 3]};
      *(LAS f32x4*)(mine + (j * 4 + g) * 1024) = v;
    }
  wait_lgkm0();
  bar();
#pragma unroll
  for (int j = 0; j < 2; ++j) {
    f32x16 keep = kh ? acc[1][j] : acc[0][j];
#pragma unroll
    for (int g = 0; g < 4; ++g) {
      f32x4 v = *(LAS f32x4*)(theirs + (j * 4 + g) * 1024);
      keep[4 * g] += v[0]; keep[4 * g + 1] += v[1]; keep[4 * g + 2] += v[2]; keep[4 * g + 3] += v[3];
    }
    acc[0][j] = keep;
  }
}

#define STAMP(i)
#define STAMP0(i)

#define OUTST 2
DI void out_store(float* p, float v) {
  __hip_atomic_store((GAS float*)p, v, RLX_AGENT);
}

template <int AUX = GATHER_AUX> DI void kv_load8(u32x4 (&v)[8], __amdgpu_buffer_rsrc_t wsr, u32 vo) {
#pragma unroll
  for (int k = 0; k < 8; ++k) v[k] = __builtin_amdgcn_raw_buffer_load_b128(wsr, vo, k * (u32)KVP_BYTES, AUX);
}
DI void kv_check8(u32x4 (&v)[8], __amdgpu_buffer_rsrc_t wsr, u32 vo, u32 tg) {
  const u64 t0 = __builtin_amdgcn_s_memrealtime();
  for (;;) {
    u32 bad = 0;
#pragma unroll
    for (int k = 0; k < 8; ++k) bad |= (v[k][1] ^ tg) | (v[k][3] ^ tg);
    if (__all(bad == 0)) break;
    if (__builtin_amdgcn_s_memrealtime() - t0 > 500000ull) break;
    __builtin_amdgcn_s_sleep(4);
    kv_load8<16>(v, wsr, vo);
  }
}
DI u32x2 kv_sum8(const u32x4 (&v)[8]) {
  f32x4 s0 = {0.f, 0.f, 0.f, 0.f};
#pragma unroll
  for (int k = 0; k < 8; ++k)
    s0 += (f32x4){__uint_as_float(v[k][0] << 16), __uint_as_float(v[k][0] & 0xffff0000u),
                  __uint_as_float(v[k][2] << 16), __uint_as_float(v[k][2] & 0xffff0000u)};
  return (u32x2){pk2(s0[0], s0[1]), pk2(s0[2], s0[3])};
}

DI void zero16(f32x16& v) {
#pragma unroll
  for (int i = 0; i < 16; ++i) v[i] = 0.f;
}

constexpr int LDS_WPHI = P1_WOFF + 2 * 12288;
constexpr int LDS_IMG = LDS_WPHI + 4096;
constexpr int LDS_RED = 32768;
constexpr int LDS_KVB = 0, LDS_ZB = 8192;
constexpr int LDS_CONST = 147456;
constexpr int LDS_E = LDS_CONST + 2048;
constexpr int LDS_HIN = LDS_E + 4096;

__global__ __launch_bounds__(768, 3) void fused_kernel(Params P) {
  extern __shared__ __attribute__((aligned(16))) char smem_raw[];
  LAS char* lds = (LAS char*)smem_raw;
  const int tid = threadIdx.x, lane = tid & 63;
  const int w = __builtin_amdgcn_readfirstlane(tid >> 6);
  const int h = lane >> 5, rho = lane & 31;
  const int bid = blockIdx.x, xcd = bid & 7, idx = bid >> 3;
  const bool isA = idx < 16;
  const int li = idx & 15;
  const int c = 4 * (xcd & 1) + (li & 3), cg = 4 * (xcd >> 1) + (li >> 2);
  const int rb = 4 * (xcd & 3) + (li & 3), nb = 4 * (xcd >> 2) + (li >> 2);
  char* ws = P.ws;
  STAMP0(0);
  const __amdgpu_buffer_rsrc_t wsr = __builtin_amdgcn_make_buffer_rsrc(ws, 0, (int)WS_END, 0x00020000);
  GAS u64* epoch = (GAS u64*)(ws + (isA ? WS_EPOCH_A : WS_EPOCH_B));

  if (w >= 8) {
    const int lw = w - 8;
    const u32 ldsb = (u32)(size_t)lds;
    __builtin_amdgcn_s_setprio(3);
    u64 epoch_val = __hip_atomic_load(epoch, RLX_AGENT);
    if (isA) {
      const int hp = cg;
      p1_load<true>(ldsb, (const char*)(P.Ax + (size_t)c * 256 * 1024), (const char*)(P.wqkv + hp * 64), nullptr, lw, lane, ROTA * (2 * xcd),
                    [&](auto) { dma_s((u32)(lane * 16), (const char*)(P.wphi + hp * 2048) + lw * 1024, ldsb + LDS_WPHI + lw * 1024); }, (u32*)(P.ws + (33u << 20)) + bid * 32);
      asm volatile("" : "+v"(epoch_val));
      const u64 tag = next_tag(epoch_val);
      bar();
      {
        const u32 tg = (u32)tag;
        const u32 kvp0 = (u32)(WS_KVP + (size_t)(hp * 8) * KVP_BYTES);
        const u64 t0 = __builtin_amdgcn_s_memrealtime();
        __builtin_amdgcn_s_setprio(0);
        for (;;) {
          const u64 v = __hip_atomic_load((const GAS u64*)(ws + kvp0 + (lane & 7) * (u32)KVP_BYTES + 1024 * 16), RLX_AGENT);
          if (__all((u32)(v >> 32) == tg)) break;
          if (__builtin_amdgcn_s_memrealtime() - t0 > 500000ull) break;
          __builtin_amdgcn_s_sleep(HINT_SLEEP);
        }
        if (lw == 0) STAMP(10);
        const u32 u0 = 512 + lw * 64 + lane;
        const u32 vo = kvp0 + u0 * 16;
        u32x4 v0[8], v1[8], vz[8];
        kv_load8(v0, wsr, vo); kv_load8(v1, wsr, vo + 4096);
        if (lw == 0) kv_load8(vz, wsr, vo + 8192);
        kv_check8(v0, wsr, vo, tg);
        *(LAS u32x2*)(lds + LDS_KVB + u0 * 8) = kv_sum8(v0);
        kv_check8(v1, wsr, vo + 4096, tg);
        *(LAS u32x2*)(lds + LDS_KVB + (u0 + 256) * 8) = kv_sum8(v1);
        if (lw == 0) {
          kv_check8(vz, wsr, vo + 8192, tg);
          float a = 0.f, b = 0.f;
#pragma unroll
          for (int k = 0; k < 8; ++k) { a += __uint_as_float(vz[k][0]); b += __uint_as_float(vz[k][2]); }
          *(LAS f32x2*)(lds + LDS_ZB + lane * 8) = (f32x2){a, b};
        }
        wait_lgkm0();
        if (lw == 0) STAMP(11);
      }
      __builtin_amdgcn_s_setprio(3);
      bar(); bar();
      if (lw == 0 && lane == 0) __hip_atomic_store((GAS u64*)(ws + WS_OFLAG + c * 128 + hp * 8), tag, RLX_AGENT);
      p2_load<true>(ldsb, ws + WS_O + (size_t)rb * 4 * 16 * 4096, nullptr, (const char*)(P.wo + nb * 128), lw, lane, ROT2 * (2 * xcd),
                    ws + WS_OFLAG + (rb >> 1) * 128, tag, (rb == 0 && nb == 0 && lw == 0) ? epoch : nullptr);
    } else {
      const int pg = cg;
      p1_load<false>(ldsb, (const char*)(P.Bx + (size_t)c * 256 * 1024), (const char*)(P.bre + (size_t)pg * 64 * 1024),
                     (const char*)(P.bim + (size_t)pg * 64 * 1024), lw, lane, ROTB * (2 * xcd + 1), [](auto) {}, (u32*)(P.ws + (33u << 20)) + bid * 32);
      asm volatile("" : "+v"(epoch_val));
      const u64 tag = next_tag(epoch_val);
      bar();
      if (lw == 0) {
        const f32x2 k1 = *(LAS f32x2*)(lds + LDS_CONST + lane * 32 + 16);
        const float pr = k1[0], pi = k1[1];
        const u32 tg = (u32)tag;
        GAS u64* agg = (GAS u64*)(ws + WS_AGG) + (size_t)pg * 8 * 128;
        LAS f32x2* E = (LAS f32x2*)(lds + LDS_E);
        float ar = 0.f, ai = 0.f;
#pragma unroll
        for (int k = 0; k < 8; ++k) {
          const f32x2 e = E[k * 64 + lane];
          const float a = fmaf(pr, ar, fmaf(-pi, ai, e[0])), b = fmaf(pr, ai, fmaf(pi, ar, e[1]));
          ar = a; ai = b;
        }
        __hip_atomic_store(agg + c * 128 + lane, ((u64)tg << 32) | __float_as_uint(ar), RLX_AGENT);
        __hip_atomic_store(agg + c * 128 + 64 + lane, ((u64)tg << 32) | __float_as_uint(ai), RLX_AGENT);
        float br = pr, bi = pi;
#pragma unroll
        for (int i = 0; i < 3; ++i) { const float a = br * br - bi * bi, b = 2.0f * br * bi; br = a; bi = b; }
        float vr[7], vi[7];
        const u64 t0 = __builtin_amdgcn_s_memrealtime();
        for (;;) {
          u64 x[7], y[7];
#pragma unroll
          for (int k = 0; k < 7; ++k) {
            const int kk = k < c ? k : c;
            x[k] = __hip_atomic_load(agg + kk * 128 + lane, RLX_AGENT);
            y[k] = __hip_atomic_load(agg + kk * 128 + 64 + lane, RLX_AGENT);
          }
          u32 bad = 0;
#pragma unroll
          for (int k = 0; k < 7; ++k) {
            vr[k] = __uint_as_float((u32)x[k]); vi[k] = __uint_as_float((u32)y[k]);
            bad |= k < c ? (((u32)(x[k] >> 32) ^ tg) | ((u32)(y[k] >> 32) ^ tg)) : 0u;
          }
          if (__all(bad == 0)) break;
          if (__builtin_amdgcn_s_memrealtime() - t0 > 500000ull) break;
          __builtin_amdgcn_s_sleep(1);
        }
        float hr = 0.f, hi = 0.f;
#pragma unroll
        for (int k = 0; k < 7; ++k) {
          const float a = fmaf(br, hr, fmaf(-bi, hi, vr[k])), b = fmaf(br, hi, fmaf(bi, hr, vi[k]));
          if (k < c) { hr = a; hi = b; }
        }
        *(LAS f32x2*)(lds + LDS_HIN + lane * 8) = (f32x2){hr, hi};
        wait_lgkm0();
      }
      bar(); bar();
      if (lw == 0 && lane == 0) __hip_atomic_store((GAS u64*)(ws + WS_HFLAG + c * 128 + pg * 8), tag, RLX_AGENT);
      p2_load<false>(ldsb, (const char*)(P.cre + (size_t)nb * 128 * 1024), (const char*)(P.cim + (size_t)nb * 128 * 1024),
                     ws + WS_H + (size_t)rb * 2 * 64 * 4096, lw, lane, ROT2 * (4 * xcd),
                     ws + WS_HFLAG + (rb >> 1) * 128, tag, (rb == 15 && nb == 0 && lw == 0) ? epoch : nullptr);
    }
    return;
  }

  const char* pfqkv = (const char*)P.wqkv + (size_t)(idx * 8 + xcd) * 24576;
  {
    const u64 pc = __builtin_amdgcn_s_getpc();
    const u32 piece = (u32)(idx & 7) * 8192u;
    if (w == 1 && piece < CODE_PF_BYTES) prefetch_lines((u32)(lane * 128), (const char*)pc + piece, (u32)(size_t)(lds + LDS_DUMP));
  }
  if (isA) {
    const int wm = w >> 1, hh = w & 1, hp = cg;
    u32 tg;
    bf16x8 phiq[2][4];
    {
      f32x16 acc[2][3];
#pragma unroll
      for (int m = 0; m < 2; ++m)
#pragma unroll
        for (int j = 0; j < 3; ++j) zero16(acc[m][j]);
      const int gb = xcd * 16 + li;
      const u32 dump = (u32)(size_t)(lds + LDS_DUMP);
      const u32 pfq = (u32)min(lane, w == 3 ? 23 : 15) << (w == 2 ? 11 : 7);
      const u32 pfl = (u32)(lane * 128);
      const char* pfx = (const char*)P.Ax + (size_t)gb * (16 * 2048);
      const char* pfw = (const char*)P.wqkv + (size_t)gb * 3072;
      const char* pfo = (const char*)P.wo + (size_t)gb * 16384;
      auto pf_tile = [&](int T) {
        if (w == 2) prefetch_lines(pfq, pfx + T * 128, dump);
        if (w == 3) prefetch_lines(pfq, pfw + (size_t)T * (64 * 6144), dump);
      };
#pragma unroll
      for (int T = 3; T < PFD; ++T) pf_tile(T);
      tg = __builtin_amdgcn_readfirstlane((u32)next_tag(__hip_atomic_load(epoch, RLX_AGENT)));
      p1_mma<true>(lds, acc, w, lane, [&]() { STAMP0(1); },
                   [&](auto T_) {
                     constexpr int T = decltype(T_)::value;
                     if constexpr (PFIN == 0 && T + PFD < 16) pf_tile(T + PFD);
                     if constexpr (WPF == 2 && T < 3) { if (w == 5) prefetch_lines(pfl, pfqkv + T * 8192, dump); }
                     if constexpr (T >= PFT && T < PFT + 8) { if (w == 4) prefetch_lines(pfq, pfo + (T - PFT) * 2048, dump); }
                   });
      STAMP0(2);
      const int blk = (lane >> 4) & 1, q = (lane & 15) >> 2, p = lane & 3;
      bf16x8 wph[2];
#pragma unroll
      for (int t = 0; t < 2; ++t)
        wph[t] = lds_frag_tr(lds + LDS_WPHI + hh * 2048 + (16 * t + 4 * h + q) * 64 + (16 * blk + 4 * p) * 2, 8 * 64);
      f32x16 kv[2];
      zero16(kv[0]); zero16(kv[1]);
      float z[2] = {0.f, 0.f};
      LAS char* img = lds + LDS_IMG + w * 4352;
      const u32 ones = 0x3f803f80u;
#pragma unroll
      for (int m = 0; m < 2; ++m) {
        f32x16 pp, ep, em;
        zero16(pp);
        pp = MFMA(wph[0], pack8(acc[m][1], 0), pp);
        pp = MFMA(wph[1], pack8(acc[m][1], 1), pp);
        dual_softmax(pp, ep, em);
#pragma unroll
        for (int g = 0; g < 4; ++g) {
          u32x2 a = {pk2(ep[4 * g], ep[4 * g + 1]), pk2(ep[4 * g + 2], ep[4 * g + 3])};
          u32x2 b = {pk2(em[4 * g], em[4 * g + 1]), pk2(em[4 * g + 2], em[4 * g + 3])};
          *(LAS u32x2*)(img + rho * 136 + (8 * g + 4 * h) * 2) = a;
          *(LAS u32x2*)(img + rho * 136 + (32 + 8 * g + 4 * h) * 2) = b;
        }
#pragma unroll
        for (int t = 0; t < 2; ++t) {
          const bf16x8 vf = pack8(acc[m][2], t);
#pragma unroll
          for (int F = 0; F < 2; ++F) {
            const bf16x8 af = lds_frag_tr(img + (16 * t + 4 * h + q) * 136 + (32 * F + 16 * blk + 4 * p) * 2, 8 * 136);
            kv[F] = MFMA(af, vf, kv[F]);
            const u32x4 au = __builtin_bit_cast(u32x4, af);
#pragma unroll
            for (int e = 0; e < 4; ++e) asm("v_dot2c_f32_bf16 %0, %1, %2" : "+v"(z[F]) : "v"(au[e]), "v"(ones));
          }
        }
      }
      STAMP0(12);
      {
        LAS char* red = lds + LDS_RED + w * 8192;
#pragma unroll
        for (int F = 0; F < 2; ++F) {
#pragma unroll
          for (int g = 0; g < 4; ++g) {
            f32x4 v = {kv[F][4 * g], kv[F][4 * g + 1], kv[F][4 * g + 2], kv[F][4 * g + 3]};
            *(LAS f32x4*)(red + ((F * 4 + g) * 64 + lane) * 16) = v;
          }
          const float zt = xhalf_sum(z[F]);
          if (h == 0) *(LAS float*)(img + (F * 32 + rho) * 4) = zt;
        }
      }
      wait_lgkm0();
      bar();
      STAMP0(13);
      const u32 kvp_me = (u32)(WS_KVP + (size_t)(hp * 8 + c) * KVP_BYTES);
#pragma unroll
      for (int e = 0; e < 2; ++e) {
        f32x4 s = *(LAS f32x4*)(lds + LDS_RED + e * 8192 + tid * 16);
#pragma unroll
        for (int k = 1; k < 4; ++k) s += *(LAS f32x4*)(lds + LDS_RED + (2 * k + e) * 8192 + tid * 16);
        const u32x4 g = {pk2(s[0], s[1]), tg, pk2(s[2], s[3]), tg};
        __builtin_amdgcn_raw_buffer_store_b128(g, wsr, kvp_me + (e * 512 + tid) * 16, 0, 16);
      }
      if (w == 7) {
        f32x2 s = *(LAS f32x2*)(lds + LDS_IMG + h * 4352 + rho * 8);
#pragma unroll
        for (int k = 1; k < 4; ++k) s += *(LAS f32x2*)(lds + LDS_IMG + (2 * k + h) * 4352 + rho * 8);
        const u32x4 g = {__float_as_uint(s[0]), tg, __float_as_uint(s[1]), tg};
        __builtin_amdgcn_raw_buffer_store_b128(g, wsr, kvp_me + (1024 + lane) * 16, 0, 16);
      }
      STAMP0(3);
#pragma unroll
      for (int m = 0; m < 2; ++m) {
        f32x16 pp, ep, em;
        zero16(pp);
        pp = MFMA(wph[0], pack8(acc[m][0], 0), pp);
        pp = MFMA(wph[1], pack8(acc[m][0], 1), pp);
        dual_softmax(pp, ep, em);
        phiq[m][0] = pack8(ep, 0); phiq[m][1] = pack8(ep, 1); phiq[m][2] = pack8(em, 0); phiq[m][3] = pack8(em, 1);
      }
    }
    STAMP0(4);
    {
      const u32 kvp0 = (u32)(WS_KVP + (size_t)(hp * 8) * KVP_BYTES);
      const u64 t0 = __builtin_amdgcn_s_memrealtime();
      for (;;) {
        const u64 hv = __hip_atomic_load((const GAS u64*)(ws + kvp0 + (lane & 7) * (u32)KVP_BYTES + 1024 * 16), RLX_AGENT);
        if (__all((u32)(hv >> 32) == tg)) break;
        if (__builtin_amdgcn_s_memrealtime() - t0 > 500000ull) break;
        __builtin_amdgcn_s_sleep(HINT_SLEEP);
      }
      u32x4 v[8];
      kv_load8(v, wsr, kvp0 + tid * 16);
      kv_check8(v, wsr, kvp0 + tid * 16, tg);
      *(LAS u32x2*)(lds + LDS_KVB + tid * 8) = kv_sum8(v);
      wait_lgkm0();
    }
    LAS char* kvb = lds + LDS_KVB;
    LAS char* zb = lds + LDS_ZB;
    bar();
    STAMP0(9);
    {
      bf16x8 kvf[4];
      f32x4 zz[2][4];
#pragma unroll
      for (int F = 0; F < 2; ++F) {
#pragma unroll
        for (int t = 0; t < 2; ++t) {
          u32x2 a = *(LAS u32x2*)(kvb + hh * 4096 + ((F * 4 + 2 * t) * 64 + lane) * 8);
          u32x2 b = *(LAS u32x2*)(kvb + hh * 4096 + ((F * 4 + 2 * t + 1) * 64 + lane) * 8);
          u32x4 v = {a[0], a[1], b[0], b[1]};
          kvf[2 * F + t] = __builtin_bit_cast(bf16x8, v);
        }
#pragma unroll
        for (int g = 0; g < 4; ++g) zz[F][g] = *(LAS f32x4*)(zb + hh * 256 + (F * 32 + 8 * g + 4 * h) * 4);
      }
#pragma unroll
      for (int m = 0; m < 2; ++m) {
        f32x16 ot;
        zero16(ot);
        float den = 0.f;
#pragma unroll
        for (int i = 0; i < 4; ++i) {
          ot = MFMA(kvf[i], phiq[m][i], ot);
          const u32x4 pu = __builtin_bit_cast(u32x4, phiq[m][i]);
#pragma unroll
          for (int e = 0; e < 4; ++e) {
            const f32x4 zv = zz[i >> 1][2 * (i & 1) + (e >> 1)];
            den = fmaf(__uint_as_float(pu[e] << 16), zv[2 * (e & 1)], den);
            den = fmaf(__uint_as_float(pu[e] & 0xffff0000u), zv[2 * (e & 1) + 1], den);
          }
        }
        den = xhalf_sum(den) + 1e-6f;
        const float r = __builtin_amdgcn_rcpf(den);
        u32 d[8];
#pragma unroll
        for (int i = 0; i < 8; ++i) d[i] = pk2(ot[2 * i] * r, ot[2 * i + 1] * r);
        const u32 obase = (u32)(WS_O + (size_t)((c * 8 + wm * 2 + m) * 16 + hp) * 4096);
#pragma unroll
        for (int i = 0; i < 2; ++i) {
          auto r0 = __builtin_amdgcn_permlane32_swap(d[4 * i], d[4 * i + 2], false, false);
          auto r1 = __builtin_amdgcn_permlane32_swap(d[4 * i + 1], d[4 * i + 3], false, false);
          u32x4 v = {r0[0], r1[0], r0[1], r1[1]};
          __builtin_amdgcn_raw_buffer_store_b128(v, wsr, obase + ((4 * hh + 2 * i + h) * 32 + rho) * 16, 0, 16);
        }
      }
    }
    wait_vm<0>();
    wait_lgkm0();
    bar();
    STAMP0(5);
    f32x16 acc[2][2];
    zero16(acc[0][0]); zero16(acc[0][1]); zero16(acc[1][0]); zero16(acc[1][1]);
    p2_mma<true>(lds, acc, w, lane, [&]() { STAMP0(6); });
    STAMP0(7);
    p2_combine(lds, acc, w, lane);
    const int kh = w >> 2, wx = (w >> 1) & 1, wy = w & 1;
    float* op = P.out + (size_t)(rb * 128 + wx * 64 + kh * 32 + 4 * h) * 2048 + nb * 128 + wy * 64 + rho;
#pragma unroll
    for (int r = 0; r < 16; ++r)
#pragma unroll
      for (int j = 0; j < 2; ++j) out_store(op + ((r & 3) + 8 * (r >> 2)) * 2048 + 32 * j, acc[0][j][r]);
    STAMP0(8);
  } else {
    const int wm = w >> 1, pn = w & 1, pg = cg, pl = pn * 32 + rho;
    const int kh = w >> 2, wx = (w >> 1) & 1, wy = w & 1;
    const int d = nb * 128 + wx * 64 + kh * 32 + rho, t0 = rb * 128 + wy * 64 + 4 * h;
    u16 xv[2][16], dsk;
    {
      f32x16 acc[2][2];
      zero16(acc[0][0]); zero16(acc[0][1]); zero16(acc[1][0]); zero16(acc[1][1]);
      const int gb = xcd * 16 + li;
      const u32 dump = (u32)(size_t)(lds + LDS_DUMP);
      const u32 pfq = (u32)min(lane, w == 4 ? 31 : 15) << (w == 4 ? 7 : 11);
      const u32 pfl = (u32)(lane * 128);
      const char* pfx = (const char*)P.Bx + (size_t)gb * (16 * 2048);
      const char* pfw = (const char*)((gb & 64) ? P.bim : P.bre) + (size_t)(gb & 63) * (16 * 2048);
      const char* pfo = (const char*)((gb & 64) ? P.cim : P.cre) + (size_t)(gb & 63) * 32768;
      auto pf_tile = [&](int T) {
        if (w == 2) prefetch_lines(pfq, pfx + T * 128, dump);
        if (w == 3) prefetch_lines(pfq, pfw + T * 128, dump);
      };
#pragma unroll
      for (int T = 3; T < PFD; ++T) pf_tile(T);
      if (w == 7) {
        const int pidx = pg * 64 + lane;
        const float la = bf2f(P.lre[pidx]), lb = bf2f(P.lim[pidx]), ldt = bf2f(P.logdt[pidx]);
        const float dt = bf2f((u16)(pk2(expf(ldt), 0.f) & 0xffffu));
        const float er = expf(dt * la);
        float sn, cs;
        sincosf(dt * lb, &sn, &cs);
        const float lr = er * cs, li_ = er * sn;
        const float nr = lr - 1.0f, dn = 1.0f / (la * la + lb * lb);
        const float c2r = 2.0f * (nr * la + li_ * lb) * dn, c2i = 2.0f * (li_ * la - nr * lb) * dn;
        float pr = lr, pi = li_;
#pragma unroll
        for (int i = 0; i < 5; ++i) { const float a = pr * pr - pi * pi, b = 2.0f * pr * pi; pr = a; pi = b; }
        *(LAS f32x4*)(lds + LDS_CONST + lane * 32) = (f32x4){lr, li_, c2r, c2i};
        *(LAS f32x2*)(lds + LDS_CONST + lane * 32 + 16) = (f32x2){pr, pi};
        wait_lgkm0();
      }
      p1_mma<false>(lds, acc, w, lane, [&]() { STAMP0(1); },
                    [&](auto T_) {
                      constexpr int T = decltype(T_)::value;
                      if constexpr (PFINB == 0 && T + PFD < 16) pf_tile(T + PFD);
                      if constexpr (WPF == 2 && T < 3) { if (w == 5) prefetch_lines(pfl, pfqkv + T * 8192, dump); }
                      if constexpr (T >= PFT && T < PFT + 8) { if (w == 4) prefetch_lines(pfq, pfo + (T - PFT) * 4096, dump); }
                    });
      STAMP0(2);
      {
        const u16* xp = P.Bx + (size_t)t0 * 1024 + d;
#pragma unroll
        for (int j = 0; j < 2; ++j)
#pragma unroll
          for (int r = 0; r < 16; ++r) xv[j][r] = xp[(32 * j + (r & 3) + 8 * (r >> 2)) * 1024];
        dsk = P.dskip[d];
      }
      const f32x4 k0 = *(LAS f32x4*)(lds + LDS_CONST + pl * 32);
      const f32x2 k1 = *(LAS f32x2*)(lds + LDS_CONST + pl * 32 + 16);
      const float lr = k0[0], li_ = k0[1], c2r = k0[2], c2i = k0[3], pr = k1[0], pi = k1[1];
      float gr = 0.f, gi = 0.f;
#pragma unroll
      for (int m = 0; m < 2; ++m)
#pragma unroll
        for (int r = 0; r < 16; ++r) {
          const float a = fmaf(lr, gr, fmaf(-li_, gi, acc[m][0][r]));
          const float b = fmaf(lr, gi, fmaf(li_, gr, acc[m][1][r]));
          gr = a; gi = b;
        }
      const int sg = 2 * wm + h;
      LAS f32x2* E = (LAS f32x2*)(lds + LDS_E);
      E[sg * 64 + pl] = (f32x2){gr, gi};
      wait_lgkm0();
      bar();
      STAMP0(3);
      float qr = 0.f, qi = 0.f, wr_ = 1.f, wi_ = 0.f;
#pragma unroll
      for (int k = 0; k < 7; ++k) {
        const f32x2 e = E[k * 64 + pl];
        const float a = fmaf(pr, qr, fmaf(-pi, qi, e[0])), b = fmaf(pr, qi, fmaf(pi, qr, e[1]));
        const float a2 = wr_ * pr - wi_ * pi, b2 = wr_ * pi + wi_ * pr;
        const bool take = k < sg;
        qr = take ? a : qr; qi = take ? b : qi; wr_ = take ? a2 : wr_; wi_ = take ? b2 : wi_;
      }
      bar();
      STAMP0(4);
      {
        const f32x2 hin = *(LAS f32x2*)(lds + LDS_HIN + pl * 8);
        gr = fmaf(wr_, hin[0], fmaf(-wi_, hin[1], qr));
        gi = fmaf(wr_, hin[1], fmaf(wi_, hin[0], qi));
      }
      const u32 hbase = (u32)(WS_H + (size_t)((((c * 4 + wm) * 16 + pg) * 2) * 2 + pn) * 4096) + (u32)(((rho >> 2) * 8 + 4 * h) * 64 + (rho & 3) * 16);
#pragma unroll
      for (int m = 0; m < 2; ++m)
#pragma unroll
        for (int half = 0; half < 2; ++half) {
          float hr[8], hi[8];
#pragma unroll
          for (int r = 0; r < 8; ++r) {
            const float a = fmaf(lr, gr, fmaf(-li_, gi, acc[m][0][8 * half + r]));
            const float b = fmaf(lr, gi, fmaf(li_, gr, acc[m][1][8 * half + r]));
            gr = a; gi = b;
            hr[r] = c2r * a - c2i * b;
            hi[r] = -(c2r * b + c2i * a);
          }
          u32x4 vr = {pk2(hr[0], hr[1]), pk2(hr[2], hr[3]), pk2(hr[4], hr[5]), pk2(hr[6], hr[7])};
          u32x4 vi = {pk2(hi[0], hi[1]), pk2(hi[2], hi[3]), pk2(hi[4], hi[5]), pk2(hi[6], hi[7])};
          const u32 o = hbase + (2 * m + half) * 64;
          __builtin_amdgcn_raw_buffer_store_b128(vr, wsr, o, 0, 16);
          __builtin_amdgcn_raw_buffer_store_b128(vi, wsr, o + 8192, 0, 16);
        }
    }
    wait_vm<0>();
    wait_lgkm0();
    bar();
    STAMP0(5);
    f32x16 acc[2][2];
#pragma unroll
    for (int j = 0; j < 2; ++j)
#pragma unroll
      for (int r = 0; r < 16; ++r) {
        const float v = bf2f(xv[j][r]) * bf2f(dsk);
        acc[0][j][r] = kh ? 0.f : v;
        acc[1][j][r] = kh ? v : 0.f;
      }
    p2_mma<false>(lds, acc, w, lane, [&]() { STAMP0(6); });
    STAMP0(7);
    p2_combine(lds, acc, w, lane);
    float* op = P.out + (size_t)t0 * 2048 + 1024 + d;
#pragma unroll
    for (int j = 0; j < 2; ++j)
#pragma unroll
      for (int r = 0; r < 16; ++r) out_store(op + (32 * j + (r & 3) + 8 * (r >> 2)) * 2048, acc[0][j][r]);
    STAMP0(8);
  }
}

extern "C" void kernel_launch(void* const* d_in, const int* in_sizes, int n_in,
                              void* d_out, int out_size, void* d_ws, size_t ws_size,
                              hipStream_t stream) {
  Params P;
  P.Ax = (const u16*)d_in[0]; P.wqkv = (const u16*)d_in[1]; P.wphi = (const u16*)d_in[2]; P.wo = (const u16*)d_in[3];
  P.Bx = (const u16*)d_in[4]; P.lre = (const u16*)d_in[5]; P.lim = (const u16*)d_in[6];
  P.bre = (const u16*)d_in[7]; P.bim = (const u16*)d_in[8]; P.cre = (const u16*)d_in[9]; P.cim = (const u16*)d_in[10];
  P.logdt = (const u16*)d_in[11]; P.dskip = (const u16*)d_in[12];
  P.out = (float*)d_out; P.ws = (char*)d_ws;
  (void)hipFuncSetAttribute((const void*)fused_kernel, hipFuncAttributeMaxDynamicSharedMemorySize, LDS_BYTES);
  fused_kernel<<<256, 768, LDS_BYTES, stream>>>(P);
}
```

```cpp
#include <hip/hip_runtime.h>
#include <hip/hip_bf16.h>

typedef unsigned short u16;
typedef unsigned int u32;
typedef unsigned long long u64;
typedef __bf16 bf16x8 __attribute__((ext_vector_type(8)));
typedef __bf16 bf16x2 __attribute__((ext_vector_type(2)));
typedef short s16x4 __attribute__((ext_vector_type(4)));
typedef short s16x8 __attribute__((ext_vector_type(8)));
typedef float f32x16 __attribute__((ext_vector_type(16)));
typedef float f32x4 __attribute__((ext_vector_type(4)));
typedef float f32x2 __attribute__((ext_vector_type(2)));
typedef u32 u32x4 __attribute__((ext_vector_type(4)));
typedef u32 u32x2 __attribute__((ext_vector_type(2)));
#define LAS __attribute__((address_space(3)))
#define GAS __attribute__((address_space(1)))
#define DI __device__ __forceinline__
#define RLX_AGENT __ATOMIC_RELAXED, __HIP_MEMORY_SCOPE_AGENT

constexpr size_t WS_EPOCH_A = 0;
constexpr size_t WS_EPOCH_B = 256;
constexpr size_t WS_KVFLAG = 4096;
constexpr size_t WS_OFLAG = WS_KVFLAG + 16 * 128;
constexpr size_t WS_HFLAG = WS_OFLAG + 8 * 128;
constexpr size_t WS_AGG = 65536;
constexpr size_t WS_KVP = WS_AGG + 16 * 8 * 1024;
constexpr size_t KVP_BYTES = (1024 + 64) * 16;
constexpr size_t WS_O = 4u << 20;
constexpr size_t WS_H = 8u << 20;
constexpr size_t WS_END = 16u << 20;

constexpr int LDS_BYTES = 163840;

struct Params {
  const u16* Ax; const u16* wqkv; const u16* wphi; const u16* wo;
  const u16* Bx; const u16* lre; const u16* lim; const u16* bre; const u16* bim;
  const u16* cre; const u16* cim; const u16* logdt; const u16* dskip;
  float* out; char* ws;
};

DI float bf2f(u16 v) { return __uint_as_float(((u32)v) << 16); }
DI u32 pk2(float a, float b) {
  f32x2 v = {a, b};
  bf16x2 r = __builtin_convertvector(v, bf16x2);
  return __builtin_bit_cast(u32, r);
}
DI bf16x8 pack8(const f32x16& x, int s) {
  u32x4 p;
  p[0] = pk2(x[8 * s + 0], x[8 * s + 1]); p[1] = pk2(x[8 * s + 2], x[8 * s + 3]);
  p[2] = pk2(x[8 * s + 4], x[8 * s + 5]); p[3] = pk2(x[8 * s + 6], x[8 * s + 7]);
  return __builtin_bit_cast(bf16x8, p);
}
#define MFMA(a, b, c) __builtin_amdgcn_mfma_f32_32x32x16_bf16((a), (b), (c), 0, 0, 0)
#define LMFMA MFMA
DI bf16x8 lds_frag(LAS char* p) { return *(LAS bf16x8*)p; }
DI s16x4 lds_tr(LAS char* __restrict__ p) { return __builtin_amdgcn_ds_read_tr16_b64_v4i16((LAS s16x4*)p); }
DI bf16x8 lds_frag_tr(LAS char* p, int off2) {
  s16x4 lo = lds_tr(p), hi = lds_tr(p + off2);
  s16x8 v = __builtin_shufflevector(lo, hi, 0, 1, 2, 3, 4, 5, 6, 7);
  return __builtin_bit_cast(bf16x8, v);
}
template <int OFF = 0> DI void dma_s(u32 voff, const char* sbase, u32 ldsaddr) {
  asm volatile("s_mov_b32 m0, %2\n\ts_nop 0\n\tglobal_load_lds_dwordx4 %0, %1 offset:%3" ::"v"(voff), "s"(sbase), "s"(ldsaddr), "n"(OFF) : "memory");
}
DI void dma_s4_dev(u32 voff, const char* sbase, u32 ldsaddr) {
  asm volatile("s_mov_b32 m0, %2\n\ts_nop 0\n\tglobal_load_lds_dwordx4 %0, %1 sc1\n\tglobal_load_lds_dwordx4 %0, %1 offset:1024 sc1\n\t"
               "global_load_lds_dwordx4 %0, %1 offset:2048 sc1\n\tglobal_load_lds_dwordx4 %0, %1 offset:3072 sc1"
               ::"v"(voff), "s"(sbase), "s"(ldsaddr) : "memory");
}
template <int N> DI void wait_vm() { asm volatile("s_waitcnt vmcnt(%0)" ::"n"(N) : "memory"); }
DI void wait_lgkm0() { asm volatile("" ::: "memory"); __builtin_amdgcn_s_waitcnt(0xC07F); asm volatile("" ::: "memory"); }
#define SB() __builtin_amdgcn_sched_barrier(0)
DI void bar() { asm volatile("" ::: "memory"); __builtin_amdgcn_s_barrier(); asm volatile("" ::: "memory"); }
DI float xhalf_max(float v) {
  auto r = __builtin_amdgcn_permlane32_swap(__float_as_uint(v), __float_as_uint(v), false, false);
  return fmaxf(__uint_as_float(r[0]), __uint_as_float(r[1]));
}
DI float xhalf_min(float v) {
  auto r = __builtin_amdgcn_permlane32_swap(__float_as_uint(v), __float_as_uint(v), false, false);
  return fminf(__uint_as_float(r[0]), __uint_as_float(r[1]));
}
DI float xhalf_sum(float v) {
  auto r = __builtin_amdgcn_permlane32_swap(__float_as_uint(v), __float_as_uint(v), false, false);
  return __uint_as_float(r[0]) + __uint_as_float(r[1]);
}
DI u64 next_tag(u64 e) { return e * 6364136223846793005ull + 1442695040888963407ull; }

DI void wait_flags(const char* flags, int n, u64 tag, int lane) {
  const GAS u64* f = (const GAS u64*)flags + (lane < n ? lane : 0);
  const u64 t0 = __builtin_amdgcn_s_memrealtime();
  for (;;) {
    u64 v = __hip_atomic_load(f, RLX_AGENT);
    if (__all(v == tag)) break;
    if (__builtin_amdgcn_s_memrealtime() - t0 > 500000ull) break;
    __builtin_amdgcn_s_sleep(1);
  }
}

#pragma float_control(precise, off, push)
DI void dual_softmax(const f32x16& p, u32 (&op)[8], u32 (&om)[8]) {
  float mx = fmaxf(p[0], p[1]), mn = fminf(p[0], p[1]);
#pragma unroll
  for (int i = 2; i < 16; i += 2) { mx = fmaxf(fmaxf(mx, p[i]), p[i + 1]); mn = fminf(fminf(mn, p[i]), p[i + 1]); }
  mx = xhalf_max(mx); mn = xhalf_min(mn);
  const float L2E = 1.4426950408889634f;
  const f32x2 cp = {L2E, L2E}, cm = {-L2E, -L2E};
  const f32x2 bp = {-mx * L2E, -mx * L2E}, bm = {mn * L2E, mn * L2E};
  f32x2 ep[8], em[8];
#pragma unroll
  for (int i = 0; i < 8; ++i) {
    const f32x2 v = {p[2 * i], p[2 * i + 1]};
    const f32x2 a = __builtin_elementwise_fma(v, cp, bp), b = __builtin_elementwise_fma(v, cm, bm);
    ep[i] = (f32x2){__builtin_amdgcn_exp2f(a[0]), __builtin_amdgcn_exp2f(a[1])};
    em[i] = (f32x2){__builtin_amdgcn_exp2f(b[0]), __builtin_amdgcn_exp2f(b[1])};
  }
  const f32x2 s2p = ((ep[0] + ep[1]) + (ep[2] + ep[3])) + ((ep[4] + ep[5]) + (ep[6] + ep[7]));
  const f32x2 s2m = ((em[0] + em[1]) + (em[2] + em[3])) + ((em[4] + em[5]) + (em[6] + em[7]));
  const float rp = __builtin_amdgcn_rcpf(xhalf_sum(s2p[0] + s2p[1])), rm = __builtin_amdgcn_rcpf(xhalf_sum(s2m[0] + s2m[1]));
  const f32x2 rp2 = {rp, rp}, rm2 = {rm, rm};
#pragma unroll
  for (int i = 0; i < 8; ++i) {
    const f32x2 a = ep[i] * rp2, b = em[i] * rm2;
    op[i] = pk2(a[0], a[1]); om[i] = pk2(b[0], b[1]);
  }
}
#pragma float_control(pop)
DI bf16x8 frag4(u32 a, u32 b, u32 c, u32 d) { const u32x4 v = {a, b, c, d}; return __builtin_bit_cast(bf16x8, v); }

#define FRONT -1
template <int NM, int ND> DI void interleave() {
  constexpr int FR = FRONT > 0 ? FRONT : NM + FRONT;
  constexpr int DPM = (ND + FR - 1) / FR;
#pragma unroll
  for (int i = 0; i < NM; ++i) {
    __builtin_amdgcn_sched_group_barrier(0x008, 1, 0);
    if (i * DPM < ND) __builtin_amdgcn_sched_group_barrier(0x100, DPM, 0);
  }
}
#define IC(n) std::integral_constant<int, (n)>{}
constexpr int LDS_DUMP = 163584;
DI void prefetch_lines(u32 voff, const char* sbase, u32 dump) {
  asm volatile("s_mov_b32 m0, %2\n\ts_nop 0\n\tglobal_load_lds_dword %0, %1" ::"v"(voff), "s"(sbase), "s"(dump) : "memory");
}

constexpr int P1_WOFF = 98304;
#define HINT_SLEEP 4
#define SPEC 40
#define OUT_AUX 16
#define GATHER_AUX 16
constexpr u32 CODE_PF_LINES = 54;
#define PFC 99
#define PFT 6
#define PFIN 0
#define PFD 5
#define ROTA 0
#define ROTB 0
#define WPF 0
#define PFINB 0
#define ROT2 1

template <int N, class F, int I = 0> DI void static_for(F&& f) {
  if constexpr (I < N) { f(std::integral_constant<int, I>{}); static_for<N, F, I + 1>(static_cast<F&&>(f)); }
}

template <bool ISA, class HOOK>
DI void p1_load(u32 ldsb, const char* xg, const char* wg0, const char* wg1, int lw, int lane, int rot, HOOK&& hook, u32* ttout = nullptr) {
  constexpr int WSLOT = ISA ? 12288 : 16384, NW = ISA ? 5 : 3;
  const u32 offE = (u32)((lane >> 3) * 2048 + (((lane & 7) ^ (lane >> 4)) * 16));
  const u32 offO = (u32)((8 + (lane >> 3)) * 2048 + (((lane & 7) ^ (4 + (lane >> 4))) * 16));
  const u32 offK = (u32)((lane >> 3) * 6144 + (((lane & 7) ^ (((lane >> 4) & 1) << 2)) * 16));
  const char* xb = xg + lw * (64 * 2048);
  const char* wb = ISA ? wg0 + lw * (8 * 6144) : (lw < 2 ? wg0 : wg1) + (lw & 1) * (32 * 2048);
  auto issue_x = [&](int T, int so) {
    const u32 l = ldsb + so + lw * 8192;
    const char* g = xb + ((T + rot) & 15) * 128;
#pragma unroll
    for (int a = 0; a < 4; ++a) { dma_s(offE, g + a * 32768, l + a * 2048); dma_s(offO, g + a * 32768, l + a * 2048 + 1024); }
  };
  auto issue_w = [&](int u, int so) {
    if constexpr (ISA) {
      const u32 l = ldsb + P1_WOFF + so + lw * 1024;
      const char* g = wb + (size_t)((u + 2 * rot) & 31) * (32 * 6144);
      dma_s(offK, g, l); dma_s(offK, g + 2048, l + 4096); dma_s(offK, g + 4096, l + 8192);
    } else {
      const u32 l = ldsb + P1_WOFF + so + lw * 4096;
      const char* g = wb + ((u + rot) & 15) * 128;
      dma_s(offE, g, l); dma_s(offO, g, l + 1024); dma_s(offE, g + 32768, l + 2048); dma_s(offO, g + 32768, l + 3072);
    }
  };
  auto wnext = [&](int o) { return o + WSLOT == NW * WSLOT ? 0 : o + WSLOT; };
  auto xnext = [&](int o) { return o == 65536 ? 0 : o + 32768; };
  int xso = 0, wso = 0;
  if constexpr (ISA) {
    issue_w(0, 0); issue_x(0, 0); issue_w(1, WSLOT);
    wait_vm<3>(); bar();
    issue_w(2, 2 * WSLOT); issue_x(1, 32768); issue_w(3, 3 * WSLOT); issue_w(4, 4 * WSLOT); issue_x(2, 65536);
#pragma unroll 1
    for (int T = 0; T < 13; ++T) {
      const int ws1 = wnext(wso);
      wait_vm<9 + 16>();
      bar();
      issue_w(2 * T + 5, wso);
      wait_vm<9 + 8>(); bar();
      issue_w(2 * T + 6, ws1); issue_x(T + 3, xso);
      wso = wnext(ws1); xso = xnext(xso);
    }
    wait_vm<9 + 16>(); bar();
    issue_w(31, wso);
    wait_vm<9 + 8>(); bar();
    hook(IC(0));
    wait_vm<6 + 8 + 1>(); bar();
    wait_vm<3 + 1>(); bar();
    wait_vm<0>(); bar();
  } else {
    issue_w(0, 0); issue_x(0, 0); issue_w(1, WSLOT); issue_x(1, 32768);
    wait_vm<12>(); bar();
    issue_w(2, 2 * WSLOT); issue_x(2, 65536);
#pragma unroll 1
    for (int T = 0; T < 13; ++T) {
      wait_vm<12>();
      bar();
      issue_w(T + 3, wso); issue_x(T + 3, xso);
      wso = wnext(wso); xso = xnext(xso);
    }
    wait_vm<12>(); bar();
    wait_vm<0>(); bar();
  }
}

template <bool ISA, class ST, class PF>
DI void p1_mma(LAS char* lds, f32x16 (&acc)[2][ISA ? 3 : 2], int w, int lane, ST&& st, PF&& pf) {
  constexpr int NT = ISA ? 3 : 2;
  constexpr int WSLOT = ISA ? 12288 : 16384;
  constexpr int NM = 2 * NT, ND = ISA ? 8 : 4;
  const int wm = w >> 1, wn = w & 1, h = lane >> 5, rho = lane & 31;
  const int arow = wm * 64 + (ISA ? rho : (32 * ((rho >> 2) & 1) + (rho & 3) + 4 * (rho >> 3)));
  constexpr int AM = ISA ? 32 * 128 : 16 * 128;
  const int aoff = arow * 128 + ((h ^ ((arow >> 1) & 7)) * 16);
  int boff;
  if constexpr (ISA) {
    const int blk = (lane >> 4) & 1, q = (lane & 15) >> 2, p = lane & 3;
    const int c = (4 * wn + 2 * blk + (p >> 1)) ^ (((q >> 1) & 1) << 2);
    boff = P1_WOFF + (8 * h + q) * 128 + c * 16 + 8 * (p & 1);
  } else {
    const int brow = wn * 32 + rho;
    boff = P1_WOFF + brow * 128 + ((h ^ ((brow >> 1) & 7)) * 16);
  }
  LAS char* pa[2][4];
  LAS char* pb[4];
#pragma unroll
  for (int s4 = 0; s4 < 4; ++s4) {
    pa[0][s4] = lds + (aoff ^ (s4 * 32)); pa[1][s4] = pa[0][s4] + 65536;
    pb[s4] = lds + (ISA ? boff : (boff ^ (s4 * 32)));
    asm volatile("" : "+v"(pa[0][s4]), "+v"(pa[1][s4]));
    if (!ISA || s4 == 0) asm volatile("" : "+v"(pb[s4]));
  }
  bf16x8 fa0[2], fb0[NT], fa1[2], fb1[NT];
  auto rd = [&](bf16x8 (&fa)[2], bf16x8 (&fb)[NT], auto T_, auto s4_) {
    constexpr int T = decltype(T_)::value, s4 = decltype(s4_)::value;
    constexpr int xs = T % 3;
    LAS char* a = pa[xs >> 1][s4] + (xs & 1) * 32768;
    fa[0] = lds_frag(a); fa[1] = lds_frag(a + AM);
    if constexpr (ISA) {
      LAS char* b = pb[0] + ((2 * T + (s4 >> 1)) % 5) * WSLOT + (s4 & 1) * 2048;
#pragma unroll
      for (int j = 0; j < 3; ++j) fb[j] = lds_frag_tr(b + j * 4096, 512);
    } else {
      LAS char* b = pb[s4] + xs * WSLOT;
      fb[0] = lds_frag(b); fb[1] = lds_frag(b + 64 * 128);
    }
  };
  auto mm = [&](bf16x8 (&fa)[2], bf16x8 (&fb)[NT]) {
#pragma unroll
    for (int m = 0; m < 2; ++m) {
      if constexpr (ISA) {
        acc[m][0] = LMFMA(fb[0], fa[m], acc[m][0]);
        acc[m][1] = LMFMA(fb[1], fa[m], acc[m][1]);
        acc[m][2] = LMFMA(fa[m], fb[2], acc[m][2]);
      } else {
        acc[m][0] = LMFMA(fa[m], fb[0], acc[m][0]);
        acc[m][1] = LMFMA(fa[m], fb[1], acc[m][1]);
      }
    }
  };
  bar();
  st();
  rd(fa0, fb0, IC(0), IC(0));
  static_for<16>([&](auto T_) {
    constexpr int T = decltype(T_)::value;
    rd(fa1, fb1, T_, IC(1)); mm(fa0, fb0); interleave<NM, ND>(); SB();
    if constexpr (ISA) { wait_lgkm0(); bar(); }
    rd(fa0, fb0, T_, IC(2)); mm(fa1, fb1); interleave<NM, ND>(); SB();
    pf(T_);
    rd(fa1, fb1, T_, IC(3)); mm(fa0, fb0); interleave<NM, ND>(); SB();
    if constexpr (T < 15) { wait_lgkm0(); bar(); rd(fa0, fb0, IC(T + 1), IC(0)); }
    mm(fa1, fb1); interleave<NM, T < 15 ? ND : 0>(); SB();
  });
}

template <bool ISA>
DI void p2_load(u32 ldsb, const char* xg0, const char* xg1, const char* yg, int lw, int lane,
                int rot, const char* flags, u64 tag, GAS u64* epoch_to_bump) {
  constexpr int NS = 4, STAGE = 32768, NK = ISA ? 16 : 32;
  const u32 lin = (u32)(lane * 16);
  u32 swE, swO;
  const char *wb0, *wb1, *hb;
  if constexpr (ISA) {
    swE = (u32)((lane >> 3) * 2048 + (((lane & 7) ^ (((lane >> 4) & 1) << 2)) * 16));
    swO = swE + 8 * 2048;
    wb0 = wb1 = yg + (lw & 1) * (32 * 2048) + (lw >> 1) * 128;
    hb = xg0 + lw * (16 * 4096);
  } else {
    swE = (u32)((lane >> 3) * 2048 + (((lane & 7) ^ (lane >> 4)) * 16));
    swO = (u32)((8 + (lane >> 3)) * 2048 + (((lane & 7) ^ (4 + (lane >> 4))) * 16));
    wb0 = xg0 + lw * (32 * 2048); wb1 = xg1 + lw * (32 * 2048);
    hb = yg + (lw >> 1) * (64 * 4096) + (lw & 1) * 4096;
  }
  auto issue_w = [&](int st, int so) {
    st = (st + rot) & (NK - 1);
    const u32 l = ldsb + so + (ISA ? 16384 : 0) + lw * 4096;
    const char* g = ISA ? wb0 + (size_t)st * (64 * 2048) : ((st & 1) ? wb1 : wb0) + (st >> 1) * 128;
    dma_s(swE, g, l); dma_s(swO, g, l + 1024); dma_s(swE, g + 32768, l + 2048); dma_s(swO, g + 32768, l + 3072);
  };
  auto issue_h = [&](int st, int so) {
    st = (st + rot) & (NK - 1);
    dma_s4_dev(lin, hb + st * (ISA ? 4096 : 8192), ldsb + so + (ISA ? 0 : 16384) + lw * 4096);
  };
#pragma unroll
  for (int s = 0; s < NS; ++s) issue_w(s, s * STAGE);
  if (lw == 0) {
    wait_flags(flags, 16, tag, lane);
    if (epoch_to_bump != nullptr && lane == 0) __hip_atomic_store(epoch_to_bump, tag, RLX_AGENT);
  }
  bar();
#pragma unroll
  for (int s = 0; s < NS; ++s) issue_h(s, s * STAGE);
  wait_vm<12>(); bar();
  int so = 0;
  auto nxt = [&]() { so = so == (NS - 1) * STAGE ? 0 : so + STAGE; };
  wait_vm<8>(); bar(); issue_h(NS + 0, so); issue_w(NS + 0, so); nxt();
  wait_vm<12>(); bar(); issue_h(NS + 1, so); issue_w(NS + 1, so); nxt();
#pragma unroll 1
  for (int t = 2; t < NK - NS; ++t) { wait_vm<16>(); bar(); issue_h(t + NS, so); issue_w(t + NS, so); nxt(); }
  wait_vm<16>(); bar(); wait_vm<8>(); bar(); wait_vm<0>(); bar();
  bar(); bar();
}

template <bool ISA, class ST>
DI void p2_mma(LAS char* lds, f32x16 (&acc)[2][2], int w, int lane, ST&& st) {
  constexpr int NS = 4, STAGE = 32768, NK = ISA ? 16 : 32;
  const int kh = w >> 2, wx = (w >> 1) & 1, wy = w & 1, h = lane >> 5, rho = lane & 31;
  const int blk = (lane >> 4) & 1, q = (lane & 15) >> 2, p = lane & 3;
  int xo, yo;
  if constexpr (ISA) {
    xo = wx * 8192 + kh * 2048 + h * 512 + rho * 16;
    yo = 16384 + wy * 8192 + (32 * kh + 8 * h + q) * 128 + (((2 * blk + (p >> 1)) ^ (((q >> 1) & 1) << 2)) * 16) + 8 * (p & 1);
  } else {
    const int xrow = wx * 64 + rho;
    xo = xrow * 128 + (((4 * kh + h) ^ ((xrow >> 1) & 7)) * 16);
    yo = 16384 + wy * 8192 + kh * 4096 + (2 * h * 8 + 2 * blk + (p >> 1)) * 64 + q * 16 + 8 * (p & 1);
  }
  LAS char* px[2][2];
  LAS char* py[2][2];
#pragma unroll
  for (int wdw = 0; wdw < 2; ++wdw) {
    px[wdw][0] = lds + wdw * 65536 + xo; px[wdw][1] = lds + wdw * 65536 + (xo ^ 32);
    py[wdw][0] = lds + wdw * 65536 + yo; py[wdw][1] = lds + wdw * 65536 + (yo ^ 64);
    asm volatile("" : "+v"(px[wdw][0]), "+v"(py[wdw][0]));
    if (ISA) asm volatile("" : "+v"(py[wdw][1])); else asm volatile("" : "+v"(px[wdw][1]));
  }
  bf16x8 fx0[2], fy0[2], fx1[2], fy1[2];
  auto rd = [&](bf16x8 (&fx)[2], bf16x8 (&fy)[2], auto slot_, auto step_) {
    constexpr int slot = decltype(slot_)::value, step = decltype(step_)::value;
    constexpr int wdw = slot >> 1, so = (slot & 1) * STAGE;
    if constexpr (ISA) {
      LAS char* x = px[wdw][0] + so + step * 1024;
      fx[0] = lds_frag(x); fx[1] = lds_frag(x + 4096);
      fy[0] = lds_frag_tr(py[wdw][0] + so + step * 2048, 512);
      fy[1] = lds_frag_tr(py[wdw][1] + so + step * 2048, 512);
    } else {
      LAS char* x = px[wdw][step] + so;
      fx[0] = lds_frag(x); fx[1] = lds_frag(x + 32 * 128);
      fy[0] = lds_frag_tr(py[wdw][0] + so + step * 2048, 512);
      fy[1] = lds_frag_tr(py[wdw][0] + so + step * 2048 + 256, 512);
    }
  };
  auto mm = [&](bf16x8 (&fx)[2], bf16x8 (&fy)[2]) {
#pragma unroll
    for (int i = 0; i < 2; ++i)
#pragma unroll
      for (int j = 0; j < 2; ++j)
        acc[i][j] = ISA ? LMFMA(fy[j], fx[i], acc[i][j]) : LMFMA(fx[i], fy[j], acc[i][j]);
  };
  bar();
  bar();
  st();
  rd(fx0, fy0, IC(0), IC(0));
  auto stage = [&](auto slot_, auto last_) {
    constexpr int slot = decltype(slot_)::value;
    constexpr bool LAST = decltype(last_)::value;
    rd(fx1, fy1, slot_, IC(1)); mm(fx0, fy0); interleave<4, 6>(); SB();
    if constexpr (!LAST) { wait_lgkm0(); bar(); rd(fx0, fy0, IC((slot + 1) % NS), IC(0)); }
    mm(fx1, fy1); interleave<4, LAST ? 0 : 6>(); SB();
  };
#pragma unroll 1
  for (int t = 0; t < NK / NS - 1; ++t) {
    stage(IC(0), std::false_type{}); stage(IC(1), std::false_type{}); stage(IC(2), std::false_type{}); stage(IC(3), std::false_type{});
  }
  stage(IC(0), std::false_type{}); stage(IC(1), std::false_type{}); stage(IC(2), std::false_type{}); stage(IC(3), std::true_type{});
}

template <bool ISA>
DI void p2_store(LAS char* lds, const f32x16 (&acc)[2][2], int w, int lane, __amdgpu_buffer_rsrc_t outr, u32 out_off, u32x2 (&xv)[8], u32x2 dsk) {
  constexpr int PITCH = 528, HALF = 128 * PITCH;
  const int kh = w >> 2, wx = (w >> 1) & 1, wy = w & 1, h = lane >> 5, rho = lane & 31;
  bar();
  LAS char* base = lds + kh * HALF + ((ISA ? wx : wy) * 64 + rho) * PITCH + ((ISA ? wy : wx) * 64 + 4 * h) * 4;
#pragma unroll
  for (int i = 0; i < 2; ++i)
#pragma unroll
    for (int j = 0; j < 2; ++j)
#pragma unroll
      for (int g = 0; g < 4; ++g) {
        const f32x4 v = {acc[i][j][4 * g], acc[i][j][4 * g + 1], acc[i][j][4 * g + 2], acc[i][j][4 * g + 3]};
        *(LAS f32x4*)(base + (ISA ? i : j) * (32 * PITCH) + ((ISA ? j : i) * 32 + 8 * g) * 4) = v;
      }
  wait_lgkm0();
  bar();
  if (!ISA) {
#pragma unroll
    for (int i = 0; i < 8; ++i) asm volatile("" : "+v"(xv[i]));
  }
  const f32x4 dv = {__uint_as_float(dsk[0] << 16), __uint_as_float(dsk[0] & 0xffff0000u), __uint_as_float(dsk[1] << 16), __uint_as_float(dsk[1] & 0xffff0000u)};
  LAS char* rp = lds + (w * 16 + h) * PITCH + rho * 16;
  const u32 vo = out_off + (u32)((w * 16 + h) * 8192 + rho * 16);
#pragma unroll
  for (int i = 0; i < 8; ++i) {
    f32x4 v = *(LAS f32x4*)(rp + 2 * i * PITCH) + *(LAS f32x4*)(rp + 2 * i * PITCH + HALF);
    if (!ISA) v += dv * (f32x4){__uint_as_float(xv[i][0] << 16), __uint_as_float(xv[i][0] & 0xffff0000u), __uint_as_float(xv[i][1] << 16), __uint_as_float(xv[i][1] & 0xffff0000u)};
    __builtin_amdgcn_raw_buffer_store_b128(__builtin_bit_cast(u32x4, v), outr, vo, i * 16384, OUT_AUX);
  }
}

#define STAMP(i)
#define STAMP0(i)

#define OUTST 2
DI void out_store(float* p, float v) {
  __hip_atomic_store((GAS float*)p, v, RLX_AGENT);
}

template <int AUX = GATHER_AUX> DI void kv_load8(u32x4 (&v)[8], __amdgpu_buffer_rsrc_t wsr, u32 vo) {
#pragma unroll
  for (int k = 0; k < 8; ++k) v[k] = __builtin_amdgcn_raw_buffer_load_b128(wsr, vo, k * (u32)KVP_BYTES, AUX);
}
DI void kv_check8(u32x4 (&v)[8], __amdgpu_buffer_rsrc_t wsr, u32 vo, u32 tg) {
  const u64 t0 = __builtin_amdgcn_s_memrealtime();
  for (;;) {
    bool any = false;
#pragma unroll
    for (int k = 0; k < 8; ++k) {
      if (!__all(v[k][1] == tg && v[k][3] == tg)) {
        v[k] = __builtin_amdgcn_raw_buffer_load_b128(wsr, vo, k * (u32)KVP_BYTES, 16);
        any = true;
      }
    }
    if (!any) break;
    if (__builtin_amdgcn_s_memrealtime() - t0 > 500000ull) break;
  }
}
DI u32x2 kv_sum8(const u32x4 (&v)[8]) {
  f32x4 s0 = {0.f, 0.f, 0.f, 0.f};
#pragma unroll
  for (int k = 0; k < 8; ++k)
    s0 += (f32x4){__uint_as_float(v[k][0] << 16), __uint_as_float(v[k][0] & 0xffff0000u),
                  __uint_as_float(v[k][2] << 16), __uint_as_float(v[k][2] & 0xffff0000u)};
  return (u32x2){pk2(s0[0], s0[1]), pk2(s0[2], s0[3])};
}

DI void zero16(f32x16& v) {
#pragma unroll
  for (int i = 0; i < 16; ++i) v[i] = 0.f;
}

constexpr int LDS_WPHI = P1_WOFF + 2 * 12288;
constexpr int LDS_IMG = LDS_WPHI + 4096;
constexpr int LDS_RED = 32768;
constexpr int LDS_KVB = 0, LDS_ZB = 8192;
constexpr int LDS_CONST = 147456;
constexpr int LDS_E = LDS_CONST + 2048;
constexpr int LDS_HIN = LDS_E + 4096;

__global__ __launch_bounds__(768, 3) void fused_kernel(Params P) {
  extern __shared__ __attribute__((aligned(16))) char smem_raw[];
  LAS char* lds = (LAS char*)smem_raw;
  u64 pc;
  asm volatile("s_getpc_b64 %0" : "=s"(pc));
  const int tid = threadIdx.x, lane = tid & 63;
  const int w = __builtin_amdgcn_readfirstlane(tid >> 6);
  const int h = lane >> 5, rho = lane & 31;
  const int bid = blockIdx.x, xcd = bid & 7, idx = bid >> 3;
  const bool isA = idx < 16;
  const int li = idx & 15;
  const int c = 4 * (xcd & 1) + (li & 3), cg = 4 * (xcd >> 1) + (li >> 2);
  const int rb = 4 * (xcd & 3) + (li & 3), nb = 4 * (xcd >> 2) + (li >> 2);
  char* ws = P.ws;
  STAMP0(0);
  const __amdgpu_buffer_rsrc_t wsr = __builtin_amdgcn_make_buffer_rsrc(ws, 0, (int)WS_END, 0x00020000);
  const __amdgpu_buffer_rsrc_t outr = __builtin_amdgcn_make_buffer_rsrc(P.out, 0, 2048 * 2048 * 4, 0x00020000);
  GAS u64* epoch = (GAS u64*)(ws + (isA ? WS_EPOCH_A : WS_EPOCH_B));

  if (__builtin_expect(w >= 8, 1)) {
    const int lw = w - 8;
    const u32 ldsb = (u32)(size_t)lds;
    __builtin_amdgcn_s_setprio(3);
    u64 epoch_val = __hip_atomic_load(epoch, RLX_AGENT);
    if (isA) {
      const int hp = cg;
      p1_load<true>(ldsb, (const char*)(P.Ax + (size_t)c * 256 * 1024), (const char*)(P.wqkv + hp * 64), nullptr, lw, lane, ROTA * (2 * xcd),
                    [&](auto) { dma_s((u32)(lane * 16), (const char*)(P.wphi + hp * 2048) + lw * 1024, ldsb + LDS_WPHI + lw * 1024); }, (u32*)(P.ws + (33u << 20)) + bid * 32);
      asm volatile("" : "+v"(epoch_val));
      const u64 tag = next_tag(epoch_val);
      bar();
      {
        const u32 tg = (u32)tag;
        const u32 kvp0 = (u32)(WS_KVP + (size_t)(hp * 8) * KVP_BYTES);
        const u64 t0 = __builtin_amdgcn_s_memrealtime();
        __builtin_amdgcn_s_setprio(0);
        __builtin_amdgcn_s_sleep(SPEC);
        const u32 u0 = 512 + lw * 64 + lane;
        const u32 vo = kvp0 + u0 * 16;
        u32x4 v0[8], v1[8], vz[8];
        kv_load8(v0, wsr, vo); kv_load8(v1, wsr, vo + 4096);
        if (lw == 0) kv_load8(vz, wsr, vo + 8192);
        kv_check8(v0, wsr, vo, tg);
        *(LAS u32x2*)(lds + LDS_KVB + u0 * 8) = kv_sum8(v0);
        kv_check8(v1, wsr, vo + 4096, tg);
        *(LAS u32x2*)(lds + LDS_KVB + (u0 + 256) * 8) = kv_sum8(v1);
        if (lw == 0) {
          kv_check8(vz, wsr, vo + 8192, tg);
          float a = 0.f, b = 0.f;
#pragma unroll
          for (int k = 0; k < 8; ++k) { a += __uint_as_float(vz[k][0]); b += __uint_as_float(vz[k][2]); }
          const u32 hi = pk2(a, b);
          const u32 lo = pk2(a - __uint_as_float(hi << 16), b - __uint_as_float(hi & 0xffff0000u));
          const int f = 2 * (lane & 15), i4 = (lane >> 4) * 2 + (f >> 4);
          LAS char* zp = lds + LDS_ZB + i4 * 64 + ((f >> 2) & 1) * 16 + (((f >> 3) & 1) * 4 + (f & 3)) * 2;
          *(LAS u32*)zp = hi; *(LAS u32*)(zp + 32) = lo;
        }
        wait_lgkm0();
        if (lw == 0) STAMP(11);
      }
      __builtin_amdgcn_s_setprio(3);
      bar(); bar();
      if (lw == 0 && lane == 0) __hip_atomic_store((GAS u64*)(ws + WS_OFLAG + c * 128 + hp * 8), tag, RLX_AGENT);
      p2_load<true>(ldsb, ws + WS_O + (size_t)rb * 4 * 16 * 4096, nullptr, (const char*)(P.wo + nb * 128), lw, lane, ROT2 * (2 * xcd),
                    ws + WS_OFLAG + (rb >> 1) * 128, tag, (rb == 0 && nb == 0 && lw == 0) ? epoch : nullptr);
    } else {
      const int pg = cg;
      p1_load<false>(ldsb, (const char*)(P.Bx + (size_t)c * 256 * 1024), (const char*)(P.bre + (size_t)pg * 64 * 1024),
                     (const char*)(P.bim + (size_t)pg * 64 * 1024), lw, lane, ROTB * (2 * xcd + 1), [](auto) {}, (u32*)(P.ws + (33u << 20)) + bid * 32);
      asm volatile("" : "+v"(epoch_val));
      const u64 tag = next_tag(epoch_val);
      bar();
      if (lw == 0) {
        const f32x2 k1 = *(LAS f32x2*)(lds + LDS_CONST + lane * 32 + 16);
        const float pr = k1[0], pi = k1[1];
        const u32 tg = (u32)tag;
        GAS u64* agg = (GAS u64*)(ws + WS_AGG) + (size_t)pg * 8 * 128;
        LAS f32x2* E = (LAS f32x2*)(lds + LDS_E);
        float ar = 0.f, ai = 0.f;
#pragma unroll
        for (int k = 0; k < 8; ++k) {
          const f32x2 e = E[k * 64 + lane];
          const float a = fmaf(pr, ar, fmaf(-pi, ai, e[0])), b = fmaf(pr, ai, fmaf(pi, ar, e[1]));
          ar = a; ai = b;
        }
        __hip_atomic_store(agg + c * 128 + lane, ((u64)tg << 32) | __float_as_uint(ar), RLX_AGENT);
        __hip_atomic_store(agg + c * 128 + 64 + lane, ((u64)tg << 32) | __float_as_uint(ai), RLX_AGENT);
        float br = pr, bi = pi;
#pragma unroll
        for (int i = 0; i < 3; ++i) { const float a = br * br - bi * bi, b = 2.0f * br * bi; br = a; bi = b; }
        float vr[7], vi[7];
        const u64 t0 = __builtin_amdgcn_s_memrealtime();
        for (;;) {
          u64 x[7], y[7];
#pragma unroll
          for (int k = 0; k < 7; ++k) {
            const int kk = k < c ? k : c;
            x[k] = __hip_atomic_load(agg + kk * 128 + lane, RLX_AGENT);
            y[k] = __hip_atomic_load(agg + kk * 128 + 64 + lane, RLX_AGENT);
          }
          u32 bad = 0;
#pragma unroll
          for (int k = 0; k < 7; ++k) {
            vr[k] = __uint_as_float((u32)x[k]); vi[k] = __uint_as_float((u32)y[k]);
            bad |= k < c ? (((u32)(x[k] >> 32) ^ tg) | ((u32)(y[k] >> 32) ^ tg)) : 0u;
          }
          if (__all(bad == 0)) break;
          if (__builtin_amdgcn_s_memrealtime() - t0 > 500000ull) break;
          __builtin_amdgcn_s_sleep(1);
        }
        float hr = 0.f, hi = 0.f;
#pragma unroll
        for (int k = 0; k < 7; ++k) {
          const float a = fmaf(br, hr, fmaf(-bi, hi, vr[k])), b = fmaf(br, hi, fmaf(bi, hr, vi[k]));
          if (k < c) { hr = a; hi = b; }
        }
        *(LAS f32x2*)(lds + LDS_HIN + lane * 8) = (f32x2){hr, hi};
        wait_lgkm0();
      }
      bar(); bar();
      if (lw == 0 && lane == 0) __hip_atomic_store((GAS u64*)(ws + WS_HFLAG + c * 128 + pg * 8), tag, RLX_AGENT);
      p2_load<false>(ldsb, (const char*)(P.cre + (size_t)nb * 128 * 1024), (const char*)(P.cim + (size_t)nb * 128 * 1024),
                     ws + WS_H + (size_t)rb * 2 * 64 * 4096, lw, lane, ROT2 * (4 * xcd),
                     ws + WS_HFLAG + (rb >> 1) * 128, tag, (rb == 15 && nb == 0 && lw == 0) ? epoch : nullptr);
    }
    return;
  }

  const char* pfqkv = (const char*)P.wqkv + (size_t)(idx * 8 + xcd) * 24576;
  const char* pfcode = (const char*)pc + (u32)(idx & 7) * (CODE_PF_LINES * 128u);
  auto pf_code = [&]() {
    if (w == 1) prefetch_lines((u32)min(lane, (int)CODE_PF_LINES - 1) << 7, pfcode, (u32)(size_t)(lds + LDS_DUMP));
  };
  pf_code();
  if (isA) {
    const int wm = w >> 1, hh = w & 1, hp = cg;
    u32 tg;
    bf16x8 phiq[2][4];
    {
      f32x16 acc[2][3];
#pragma unroll
      for (int m = 0; m < 2; ++m)
#pragma unroll
        for (int j = 0; j < 3; ++j) zero16(acc[m][j]);
      const int gb = xcd * 16 + li;
      const u32 dump = (u32)(size_t)(lds + LDS_DUMP);
      const u32 pfq = (u32)min(lane, w == 3 ? 23 : 15) << (w == 2 ? 11 : 7);
      const u32 pfl = (u32)(lane * 128);
      const char* pfx = (const char*)P.Ax + (size_t)gb * (16 * 2048);
      const char* pfw = (const char*)P.wqkv + (size_t)gb * 3072;
      const char* pfo = (const char*)P.wo + (size_t)gb * 16384;
      auto pf_tile = [&](int T) {
        if (w == 2) prefetch_lines(pfq, pfx + T * 128, dump);
        if (w == 3) prefetch_lines(pfq, pfw + (size_t)T * (64 * 6144), dump);
      };
#pragma unroll
      for (int T = 3; T < PFD; ++T) pf_tile(T);
      tg = __builtin_amdgcn_readfirstlane((u32)next_tag(__hip_atomic_load(epoch, RLX_AGENT)));
      p1_mma<true>(lds, acc, w, lane, [&]() { STAMP0(1); },
                   [&](auto T_) {
                     constexpr int T = decltype(T_)::value;
                     if constexpr (PFIN == 0 && T + PFD < 16) pf_tile(T + PFD);
                     if constexpr (T == PFC) pf_code();
                     if constexpr (WPF == 2 && T < 3) { if (w == 5) prefetch_lines(pfl, pfqkv + T * 8192, dump); }
                     if constexpr (T >= PFT && T < PFT + 8) { if (w == 4) prefetch_lines(pfq, pfo + (T - PFT) * 2048, dump); }
                   });
      STAMP0(2);
      const int blk = (lane >> 4) & 1, q = (lane & 15) >> 2, p = lane & 3;
      bf16x8 wph[2];
#pragma unroll
      for (int t = 0; t < 2; ++t)
        wph[t] = lds_frag_tr(lds + LDS_WPHI + hh * 2048 + (16 * t + 4 * h + q) * 64 + (16 * blk + 4 * p) * 2, 8 * 64);
      f32x16 kv[2];
      zero16(kv[0]); zero16(kv[1]);
      float z[2] = {0.f, 0.f};
      LAS char* img = lds + LDS_IMG + w * 4352;
      const u32 ones = 0x3f803f80u;
#pragma unroll
      for (int m = 0; m < 2; ++m) {
        f32x16 pp;
        u32 ep[8], em[8];
        zero16(pp);
        pp = MFMA(wph[0], pack8(acc[m][1], 0), pp);
        pp = MFMA(wph[1], pack8(acc[m][1], 1), pp);
        dual_softmax(pp, ep, em);
#pragma unroll
        for (int g = 0; g < 4; ++g) {
          const u32x2 a = {ep[2 * g], ep[2 * g + 1]}, b = {em[2 * g], em[2 * g + 1]};
          *(LAS u32x2*)(img + rho * 136 + (8 * g + 4 * h) * 2) = a;
          *(LAS u32x2*)(img + rho * 136 + (32 + 8 * g + 4 * h) * 2) = b;
        }
#pragma unroll
        for (int t = 0; t < 2; ++t) {
          const bf16x8 vf = pack8(acc[m][2], t);
#pragma unroll
          for (int F = 0; F < 2; ++F) {
            const bf16x8 af = lds_frag_tr(img + (16 * t + 4 * h + q) * 136 + (32 * F + 16 * blk + 4 * p) * 2, 8 * 136);
            kv[F] = MFMA(af, vf, kv[F]);
            const u32x4 au = __builtin_bit_cast(u32x4, af);
#pragma unroll
            for (int e = 0; e < 4; ++e) asm("v_dot2c_f32_bf16 %0, %1, %2" : "+v"(z[F]) : "v"(au[e]), "v"(ones));
          }
        }
      }
      STAMP0(12);
      {
        LAS char* red = lds + LDS_RED + w * 8192;
#pragma unroll
        for (int F = 0; F < 2; ++F) {
#pragma unroll
          for (int g = 0; g < 4; ++g) {
            f32x4 v = {kv[F][4 * g], kv[F][4 * g + 1], kv[F][4 * g + 2], kv[F][4 * g + 3]};
            *(LAS f32x4*)(red + ((F * 4 + g) * 64 + lane) * 16) = v;
          }
          const float zt = xhalf_sum(z[F]);
          if (h == 0) *(LAS float*)(img + (F * 32 + rho) * 4) = zt;
        }
      }
      wait_lgkm0();
      bar();
      STAMP0(13);
      const u32 kvp_me = (u32)(WS_KVP + (size_t)(hp * 8 + c) * KVP_BYTES);
#pragma unroll
      for (int e = 0; e < 2; ++e) {
        f32x4 s = *(LAS f32x4*)(lds + LDS_RED + e * 8192 + tid * 16);
#pragma unroll
        for (int k = 1; k < 4; ++k) s += *(LAS f32x4*)(lds + LDS_RED + (2 * k + e) * 8192 + tid * 16);
        const u32x4 g = {pk2(s[0], s[1]), tg, pk2(s[2], s[3]), tg};
        __builtin_amdgcn_raw_buffer_store_b128(g, wsr, kvp_me + (e * 512 + tid) * 16, 0, 16);
      }
      if (w == 7) {
        f32x2 s = *(LAS f32x2*)(lds + LDS_IMG + h * 4352 + rho * 8);
#pragma unroll
        for (int k = 1; k < 4; ++k) s += *(LAS f32x2*)(lds + LDS_IMG + (2 * k + h) * 4352 + rho * 8);
        const u32x4 g = {__float_as_uint(s[0]), tg, __float_as_uint(s[1]), tg};
        __builtin_amdgcn_raw_buffer_store_b128(g, wsr, kvp_me + (1024 + lane) * 16, 0, 16);
      }
      STAMP0(3);
#pragma unroll
      for (int m = 0; m < 2; ++m) {
        f32x16 pp;
        u32 ep[8], em[8];
        zero16(pp);
        pp = MFMA(wph[0], pack8(acc[m][0], 0), pp);
        pp = MFMA(wph[1], pack8(acc[m][0], 1), pp);
        dual_softmax(pp, ep, em);
        phiq[m][0] = frag4(ep[0], ep[1], ep[2], ep[3]); phiq[m][1] = frag4(ep[4], ep[5], ep[6], ep[7]);
        phiq[m][2] = frag4(em[0], em[1], em[2], em[3]); phiq[m][3] = frag4(em[4], em[5], em[6], em[7]);
      }
    }
    STAMP0(4);
    {
      const u32 kvp0 = (u32)(WS_KVP + (size_t)(hp * 8) * KVP_BYTES);
      const u64 t0 = __builtin_amdgcn_s_memrealtime();
      u32x4 v[8];
      kv_load8(v, wsr, kvp0 + tid * 16);
      kv_check8(v, wsr, kvp0 + tid * 16, tg);
      *(LAS u32x2*)(lds + LDS_KVB + tid * 8) = kv_sum8(v);
      wait_lgkm0();
    }
    LAS char* kvb = lds + LDS_KVB;
    LAS char* zb = lds + LDS_ZB;
    bar();
    STAMP0(9);
    {
      bf16x8 kvf[4], zf[4];
#pragma unroll
      for (int i = 0; i < 4; ++i) {
        const u32x2 a = *(LAS u32x2*)(kvb + hh * 4096 + ((2 * i) * 64 + lane) * 8);
        const u32x2 b = *(LAS u32x2*)(kvb + hh * 4096 + ((2 * i + 1) * 64 + lane) * 8);
        kvf[i] = frag4(a[0], a[1], b[0], b[1]);
        zf[i] = lds_frag(zb + (hh * 4 + i) * 64 + (rho >> 4) * 32 + h * 16);
      }
#pragma unroll
      for (int m = 0; m < 2; ++m) {
        f32x16 ot, dn;
        zero16(ot); zero16(dn);
#pragma unroll
        for (int i = 0; i < 4; ++i) { dn = MFMA(zf[i], phiq[m][i], dn); ot = MFMA(kvf[i], phiq[m][i], ot); }
        const float r = __builtin_amdgcn_rcpf((dn[0] + dn[8]) + 1e-6f);
        u32 d[8];
#pragma unroll
        for (int i = 0; i < 8; ++i) d[i] = pk2(ot[2 * i] * r, ot[2 * i + 1] * r);
        const u32 obase = (u32)(WS_O + (size_t)((c * 8 + wm * 2 + m) * 16 + hp) * 4096);
#pragma unroll
        for (int i = 0; i < 2; ++i) {
          auto r0 = __builtin_amdgcn_permlane32_swap(d[4 * i], d[4 * i + 2], false, false);
          auto r1 = __builtin_amdgcn_permlane32_swap(d[4 * i + 1], d[4 * i + 3], false, false);
          u32x4 v = {r0[0], r1[0], r0[1], r1[1]};
          __builtin_amdgcn_raw_buffer_store_b128(v, wsr, obase + ((4 * hh + 2 * i + h) * 32 + rho) * 16, 0, 16);
        }
      }
    }
    wait_vm<0>();
    wait_lgkm0();
    bar();
    STAMP0(5);
    f32x16 acc[2][2];
    zero16(acc[0][0]); zero16(acc[0][1]); zero16(acc[1][0]); zero16(acc[1][1]);
    p2_mma<true>(lds, acc, w, lane, [&]() { STAMP0(6); });
    STAMP0(7);
    u32x2 nox[8] = {};
    p2_store<true>(lds, acc, w, lane, outr, (u32)(rb * 128 * 8192 + nb * 512), nox, nox[0]);
    STAMP0(8);
  } else {
    const int wm = w >> 1, pn = w & 1, pg = cg, pl = pn * 32 + rho;
    u32x2 xv[8], dsk;
    {
      f32x16 acc[2][2];
      zero16(acc[0][0]); zero16(acc[0][1]); zero16(acc[1][0]); zero16(acc[1][1]);
      const int gb = xcd * 16 + li;
      const u32 dump = (u32)(size_t)(lds + LDS_DUMP);
      const u32 pfq = (u32)min(lane, w == 4 ? 31 : 15) << (w == 4 ? 7 : 11);
      const u32 pfl = (u32)(lane * 128);
      const char* pfx = (const char*)P.Bx + (size_t)gb * (16 * 2048);
      const char* pfw = (const char*)((gb & 64) ? P.bim : P.bre) + (size_t)(gb & 63) * (16 * 2048);
      const char* pfo = (const char*)((gb & 64) ? P.cim : P.cre) + (size_t)(gb & 63) * 32768;
      auto pf_tile = [&](int T) {
        if (w == 2) prefetch_lines(pfq, pfx + T * 128, dump);
        if (w == 3) prefetch_lines(pfq, pfw + T * 128, dump);
      };
#pragma unroll
      for (int T = 3; T < PFD; ++T) pf_tile(T);
      if (w == 7) {
        const int pidx = pg * 64 + lane;
        const float la = bf2f(P.lre[pidx]), lb = bf2f(P.lim[pidx]), ldt = bf2f(P.logdt[pidx]);
        const float dt = bf2f((u16)(pk2(expf(ldt), 0.f) & 0xffffu));
        const float er = expf(dt * la);
        float sn, cs;
        sincosf(dt * lb, &sn, &cs);
        const float lr = er * cs, li_ = er * sn;
        const float nr = lr - 1.0f, dn = 1.0f / (la * la + lb * lb);
        const float c2r = 2.0f * (nr * la + li_ * lb) * dn, c2i = 2.0f * (li_ * la - nr * lb) * dn;
        float pr = lr, pi = li_;
#pragma unroll
        for (int i = 0; i < 5; ++i) { const float a = pr * pr - pi * pi, b = 2.0f * pr * pi; pr = a; pi = b; }
        *(LAS f32x4*)(lds + LDS_CONST + lane * 32) = (f32x4){lr, li_, c2r, c2i};
        *(LAS f32x2*)(lds + LDS_CONST + lane * 32 + 16) = (f32x2){pr, pi};
        wait_lgkm0();
      }
      p1_mma<false>(lds, acc, w, lane, [&]() { STAMP0(1); },
                    [&](auto T_) {
                      constexpr int T = decltype(T_)::value;
                      if constexpr (PFINB == 0 && T + PFD < 16) pf_tile(T + PFD);
                      if constexpr (T == PFC) pf_code();
                      if constexpr (WPF == 2 && T < 3) { if (w == 5) prefetch_lines(pfl, pfqkv + T * 8192, dump); }
                      if constexpr (T >= PFT && T < PFT + 8) { if (w == 4) prefetch_lines(pfq, pfo + (T - PFT) * 4096, dump); }
                    });
      STAMP0(2);
      const f32x4 k0 = *(LAS f32x4*)(lds + LDS_CONST + pl * 32);
      const f32x2 k1 = *(LAS f32x2*)(lds + LDS_CONST + pl * 32 + 16);
      const float lr = k0[0], li_ = k0[1], c2r = k0[2], c2i = k0[3], pr = k1[0], pi = k1[1];
      float gr = 0.f, gi = 0.f;
#pragma unroll
      for (int m = 0; m < 2; ++m)
#pragma unroll
        for (int r = 0; r < 16; ++r) {
          const float a = fmaf(lr, gr, fmaf(-li_, gi, acc[m][0][r]));
          const float b = fmaf(lr, gi, fmaf(li_, gr, acc[m][1][r]));
          gr = a; gi = b;
        }
      const int sg = 2 * wm + h;
      LAS f32x2* E = (LAS f32x2*)(lds + LDS_E);
      E[sg * 64 + pl] = (f32x2){gr, gi};
      wait_lgkm0();
      bar();
      STAMP0(3);
      float qr = 0.f, qi = 0.f, wr_ = 1.f, wi_ = 0.f;
#pragma unroll
      for (int k = 0; k < 7; ++k) {
        const f32x2 e = E[k * 64 + pl];
        const float a = fmaf(pr, qr, fmaf(-pi, qi, e[0])), b = fmaf(pr, qi, fmaf(pi, qr, e[1]));
        const float a2 = wr_ * pr - wi_ * pi, b2 = wr_ * pi + wi_ * pr;
        const bool take = k < sg;
        qr = take ? a : qr; qi = take ? b : qi; wr_ = take ? a2 : wr_; wi_ = take ? b2 : wi_;
      }
      bar();
      STAMP0(4);
      {
        const f32x2 hin = *(LAS f32x2*)(lds + LDS_HIN + pl * 8);
        gr = fmaf(wr_, hin[0], fmaf(-wi_, hin[1], qr));
        gi = fmaf(wr_, hin[1], fmaf(wi_, hin[0], qi));
      }
      const u32 hbase = (u32)(WS_H + (size_t)((((c * 4 + wm) * 16 + pg) * 2) * 2 + pn) * 4096) + (u32)(((rho >> 2) * 8 + 4 * h) * 64 + (rho & 3) * 16);
#pragma unroll
      for (int m = 0; m < 2; ++m)
#pragma unroll
        for (int half = 0; half < 2; ++half) {
          float hr[8], hi[8];
#pragma unroll
          for (int r = 0; r < 8; ++r) {
            const float a = fmaf(lr, gr, fmaf(-li_, gi, acc[m][0][8 * half + r]));
            const float b = fmaf(lr, gi, fmaf(li_, gr, acc[m][1][8 * half + r]));
            gr = a; gi = b;
            hr[r] = c2r * a - c2i * b;
            hi[r] = -(c2r * b + c2i * a);
          }
          u32x4 vr = {pk2(hr[0], hr[1]), pk2(hr[2], hr[3]), pk2(hr[4], hr[5]), pk2(hr[6], hr[7])};
          u32x4 vi = {pk2(hi[0], hi[1]), pk2(hi[2], hi[3]), pk2(hi[4], hi[5]), pk2(hi[6], hi[7])};
          const u32 o = hbase + (2 * m + half) * 64;
          __builtin_amdgcn_raw_buffer_store_b128(vr, wsr, o, 0, 16);
          __builtin_amdgcn_raw_buffer_store_b128(vi, wsr, o + 8192, 0, 16);
        }
    }
    wait_vm<0>();
    wait_lgkm0();
    bar();
    STAMP0(5);
      {
        const u16* xp = P.Bx + (size_t)(rb * 128 + w * 16 + h) * 1024 + nb * 128 + rho * 4;
#pragma unroll
        for (int i = 0; i < 8; ++i) xv[i] = *(const u32x2*)(xp + 2 * i * 1024);
        dsk = *(const u32x2*)(P.dskip + nb * 128 + rho * 4);
      }
    f32x16 acc[2][2];
    zero16(acc[0][0]); zero16(acc[0][1]); zero16(acc[1][0]); zero16(acc[1][1]);
    p2_mma<false>(lds, acc, w, lane, [&]() { STAMP0(6); });
    STAMP0(7);
    p2_store<false>(lds, acc, w, lane, outr, (u32)(rb * 128 * 8192 + 4096 + nb * 512), xv, dsk);
    STAMP0(8);
  }
}

extern "C" void kernel_launch(void* const* d_in, const int* in_sizes, int n_in,
                              void* d_out, int out_size, void* d_ws, size_t ws_size,
                              hipStream_t stream) {
  Params P;
  P.Ax = (const u16*)d_in[0]; P.wqkv = (const u16*)d_in[1]; P.wphi = (const u16*)d_in[2]; P.wo = (const u16*)d_in[3];
  P.Bx = (const u16*)d_in[4]; P.lre = (const u16*)d_in[5]; P.lim = (const u16*)d_in[6];
  P.bre = (const u16*)d_in[7]; P.bim = (const u16*)d_in[8]; P.cre = (const u16*)d_in[9]; P.cim = (const u16*)d_in[10];
  P.logdt = (const u16*)d_in[11]; P.dskip = (const u16*)d_in[12];
  P.out = (float*)d_out; P.ws = (char*)d_ws;
  (void)hipFuncSetAttribute((const void*)fused_kernel, hipFuncAttributeMaxDynamicSharedMemorySize, LDS_BYTES);
  fused_kernel<<<256, 768, LDS_BYTES, stream>>>(P);
}
```

```cpp
#include <hip/hip_runtime.h>
#include <hip/hip_bf16.h>
#include <type_traits>

typedef unsigned short u16;
typedef unsigned int u32;
typedef unsigned long long u64;
typedef __bf16 bf16x8 __attribute__((ext_vector_type(8)));
typedef __bf16 bf16x2 __attribute__((ext_vector_type(2)));
typedef short s16x4 __attribute__((ext_vector_type(4)));
typedef short s16x8 __attribute__((ext_vector_type(8)));
typedef float f32x16 __attribute__((ext_vector_type(16)));
typedef float f32x4 __attribute__((ext_vector_type(4)));
typedef float f32x2 __attribute__((ext_vector_type(2)));
typedef u32 u32x4 __attribute__((ext_vector_type(4)));
typedef u32 u32x2 __attribute__((ext_vector_type(2)));
#define LAS __attribute__((address_space(3)))
#define GAS __attribute__((address_space(1)))
#define DI __device__ __forceinline__
#define RLX_AGENT __ATOMIC_RELAXED, __HIP_MEMORY_SCOPE_AGENT

constexpr size_t WS_EPOCH_A = 0;
constexpr size_t WS_EPOCH_B = 256;
constexpr size_t WS_OFLAG = 4096;
constexpr size_t WS_HFLAG = WS_OFLAG + 8 * 128;
constexpr size_t WS_AGG = 65536;
constexpr size_t WS_KVP = WS_AGG + 16 * 8 * 1024;
constexpr size_t KVP_BYTES = (1024 + 64) * 16;
constexpr size_t WS_O = 4u << 20;
constexpr size_t WS_H = 8u << 20;
constexpr size_t WS_END = 16u << 20;

constexpr int LDS_BYTES = 163840;

struct Params {
  const u16* Ax; const u16* wqkv; const u16* wphi; const u16* wo;
  const u16* Bx; const u16* lre; const u16* lim; const u16* bre; const u16* bim;
  const u16* cre; const u16* cim; const u16* logdt; const u16* dskip;
  float* out; char* ws;
};

DI float bf2f(u16 v) { return __uint_as_float(((u32)v) << 16); }
DI u32 pk2(float a, float b) {
  f32x2 v = {a, b};
  bf16x2 r = __builtin_convertvector(v, bf16x2);
  return __builtin_bit_cast(u32, r);
}
DI bf16x8 pack8(const f32x16& x, int s) {
  u32x4 p;
  p[0] = pk2(x[8 * s + 0], x[8 * s + 1]); p[1] = pk2(x[8 * s + 2], x[8 * s + 3]);
  p[2] = pk2(x[8 * s + 4], x[8 * s + 5]); p[3] = pk2(x[8 * s + 6], x[8 * s + 7]);
  return __builtin_bit_cast(bf16x8, p);
}
#define MFMA(a, b, c) __builtin_amdgcn_mfma_f32_32x32x16_bf16((a), (b), (c), 0, 0, 0)
DI bf16x8 lds_frag(LAS char* p) { return *(LAS bf16x8*)p; }
DI s16x4 lds_tr(LAS char* __restrict__ p) { return __builtin_amdgcn_ds_read_tr16_b64_v4i16((LAS s16x4*)p); }
DI bf16x8 lds_frag_tr(LAS char* p, int off2) {
  s16x4 lo = lds_tr(p), hi = lds_tr(p + off2);
  s16x8 v = __builtin_shufflevector(lo, hi, 0, 1, 2, 3, 4, 5, 6, 7);
  return __builtin_bit_cast(bf16x8, v);
}
template <int OFF = 0> DI void dma_s(u32 voff, const char* sbase, u32 ldsaddr) {
  asm volatile("s_mov_b32 m0, %2\n\ts_nop 0\n\tglobal_load_lds_dwordx4 %0, %1 offset:%3" ::"v"(voff), "s"(sbase), "s"(ldsaddr), "n"(OFF) : "memory");
}
DI void dma_s4_dev(u32 voff, const char* sbase, u32 ldsaddr) {
  asm volatile("s_mov_b32 m0, %2\n\ts_nop 0\n\tglobal_load_lds_dwordx4 %0, %1 sc1\n\tglobal_load_lds_dwordx4 %0, %1 offset:1024 sc1\n\t"
               "global_load_lds_dwordx4 %0, %1 offset:2048 sc1\n\tglobal_load_lds_dwordx4 %0, %1 offset:3072 sc1"
               ::"v"(voff), "s"(sbase), "s"(ldsaddr) : "memory");
}
template <int N> DI void wait_vm() { asm volatile("s_waitcnt vmcnt(%0)" ::"n"(N) : "memory"); }
DI void wait_lgkm0() { asm volatile("" ::: "memory"); __builtin_amdgcn_s_waitcnt(0xC07F); asm volatile("" ::: "memory"); }
#define SB() __builtin_amdgcn_sched_barrier(0)
DI void bar() { asm volatile("" ::: "memory"); __builtin_amdgcn_s_barrier(); asm volatile("" ::: "memory"); }
DI float xhalf_max(float v) {
  auto r = __builtin_amdgcn_permlane32_swap(__float_as_uint(v), __float_as_uint(v), false, false);
  return fmaxf(__uint_as_float(r[0]), __uint_as_float(r[1]));
}
DI float xhalf_min(float v) {
  auto r = __builtin_amdgcn_permlane32_swap(__float_as_uint(v), __float_as_uint(v), false, false);
  return fminf(__uint_as_float(r[0]), __uint_as_float(r[1]));
}
DI float xhalf_sum(float v) {
  auto r = __builtin_amdgcn_permlane32_swap(__float_as_uint(v), __float_as_uint(v), false, false);
  return __uint_as_float(r[0]) + __uint_as_float(r[1]);
}
DI u64 next_tag(u64 e) { return e * 6364136223846793005ull + 1442695040888963407ull; }

DI void wait_flags(const char* flags, int n, u64 tag, int lane) {
  const GAS u64* f = (const GAS u64*)flags + (lane < n ? lane : 0);
  const u64 t0 = __builtin_amdgcn_s_memrealtime();
  for (;;) {
    u64 v = __hip_atomic_load(f, RLX_AGENT);
    if (__all(v == tag)) break;
    if (__builtin_amdgcn_s_memrealtime() - t0 > 500000ull) break;
    __builtin_amdgcn_s_sleep(1);
  }
}

DI void dual_softmax(const f32x16& p, u32 (&op)[8], u32 (&om)[8]) {
  float mx = fmaxf(p[0], p[1]), mn = fminf(p[0], p[1]);
#pragma unroll
  for (int i = 2; i < 16; i += 2) { mx = fmaxf(fmaxf(mx, p[i]), p[i + 1]); mn = fminf(fminf(mn, p[i]), p[i + 1]); }
  mx = xhalf_max(mx); mn = xhalf_min(mn);
  const float L2E = 1.4426950408889634f;
  const f32x2 cp = {L2E, L2E}, cm = {-L2E, -L2E};
  const f32x2 bp = {-mx * L2E, -mx * L2E}, bm = {mn * L2E, mn * L2E};
  f32x2 ep[8], em[8];
#pragma unroll
  for (int i = 0; i < 8; ++i) {
    const f32x2 v = {p[2 * i], p[2 * i + 1]};
    const f32x2 a = __builtin_elementwise_fma(v, cp, bp), b = __builtin_elementwise_fma(v, cm, bm);
    ep[i] = (f32x2){__builtin_amdgcn_exp2f(a[0]), __builtin_amdgcn_exp2f(a[1])};
    em[i] = (f32x2){__builtin_amdgcn_exp2f(b[0]), __builtin_amdgcn_exp2f(b[1])};
  }
  const f32x2 s2p = ((ep[0] + ep[1]) + (ep[2] + ep[3])) + ((ep[4] + ep[5]) + (ep[6] + ep[7]));
  const f32x2 s2m = ((em[0] + em[1]) + (em[2] + em[3])) + ((em[4] + em[5]) + (em[6] + em[7]));
  const float rp = __builtin_amdgcn_rcpf(xhalf_sum(s2p[0] + s2p[1])), rm = __builtin_amdgcn_rcpf(xhalf_sum(s2m[0] + s2m[1]));
  const f32x2 rp2 = {rp, rp}, rm2 = {rm, rm};
#pragma unroll
  for (int i = 0; i < 8; ++i) {
    const f32x2 a = ep[i] * rp2, b = em[i] * rm2;
    op[i] = pk2(a[0], a[1]); om[i] = pk2(b[0], b[1]);
  }
}
DI bf16x8 frag4(u32 a, u32 b, u32 c, u32 d) { const u32x4 v = {a, b, c, d}; return __builtin_bit_cast(bf16x8, v); }

template <int NM, int ND> DI void interleave() {
  constexpr int DPM = (ND + NM - 2) / (NM - 1);
#pragma unroll
  for (int i = 0; i < NM; ++i) {
    __builtin_amdgcn_sched_group_barrier(0x008, 1, 0);
    if (i * DPM < ND) __builtin_amdgcn_sched_group_barrier(0x100, DPM, 0);
  }
}
#define IC(n) std::integral_constant<int, (n)>{}
constexpr int LDS_DUMP = 163584;
DI void prefetch_lines(u32 voff, const char* sbase, u32 dump) {
  asm volatile("s_mov_b32 m0, %2\n\ts_nop 0\n\tglobal_load_lds_dword %0, %1" ::"v"(voff), "s"(sbase), "s"(dump) : "memory");
}

constexpr int P1_WOFF = 98304;
constexpr u32 CODE_PF_LINES = 52;
constexpr int PFD = 5;
constexpr int PFT = 6;

template <int N, class F, int I = 0> DI void static_for(F&& f) {
  if constexpr (I < N) { f(std::integral_constant<int, I>{}); static_for<N, F, I + 1>(static_cast<F&&>(f)); }
}

template <bool ISA, class HOOK>
DI void p1_load(u32 ldsb, const char* xg, const char* wg0, const char* wg1, int lw, int lane, HOOK&& hook) {
  constexpr int WSLOT = ISA ? 12288 : 16384, NW = ISA ? 5 : 3;
  const u32 offE = (u32)((lane >> 3) * 2048 + (((lane & 7) ^ (lane >> 4)) * 16));
  const u32 offO = (u32)((8 + (lane >> 3)) * 2048 + (((lane & 7) ^ (4 + (lane >> 4))) * 16));
  const u32 offK = (u32)((lane >> 3) * 6144 + (((lane & 7) ^ (((lane >> 4) & 1) << 2)) * 16));
  const char* xb = xg + lw * (64 * 2048);
  const char* wb = ISA ? wg0 + lw * (8 * 6144) : (lw < 2 ? wg0 : wg1) + (lw & 1) * (32 * 2048);
  auto issue_x = [&](int T, int so) {
    const u32 l = ldsb + so + lw * 8192;
    const char* g = xb + T * 128;
#pragma unroll
    for (int a = 0; a < 4; ++a) { dma_s(offE, g + a * 32768, l + a * 2048); dma_s(offO, g + a * 32768, l + a * 2048 + 1024); }
  };
  auto issue_w = [&](int u, int so) {
    if constexpr (ISA) {
      const u32 l = ldsb + P1_WOFF + so + lw * 1024;
      const char* g = wb + (size_t)u * (32 * 6144);
      dma_s(offK, g, l); dma_s(offK, g + 2048, l + 4096); dma_s(offK, g + 4096, l + 8192);
    } else {
      const u32 l = ldsb + P1_WOFF + so + lw * 4096;
      const char* g = wb + u * 128;
      dma_s(offE, g, l); dma_s(offO, g, l + 1024); dma_s(offE, g + 32768, l + 2048); dma_s(offO, g + 32768, l + 3072);
    }
  };
  auto wnext = [&](int o) { return o + WSLOT == NW * WSLOT ? 0 : o + WSLOT; };
  auto xnext = [&](int o) { return o == 65536 ? 0 : o + 32768; };
  int xso = 0, wso = 0;
  if constexpr (ISA) {
    issue_w(0, 0); issue_x(0, 0); issue_w(1, WSLOT);
    wait_vm<3>(); bar();
    issue_w(2, 2 * WSLOT); issue_x(1, 32768); issue_w(3, 3 * WSLOT); issue_w(4, 4 * WSLOT); issue_x(2, 65536);
#pragma unroll 1
    for (int T = 0; T < 13; ++T) {
      const int ws1 = wnext(wso);
      wait_vm<9 + 16>();
      bar();
      issue_w(2 * T + 5, wso);
      wait_vm<9 + 8>(); bar();
      issue_w(2 * T + 6, ws1); issue_x(T + 3, xso);
      wso = wnext(ws1); xso = xnext(xso);
    }
    wait_vm<9 + 16>(); bar();
    issue_w(31, wso);
    wait_vm<9 + 8>(); bar();
    hook(IC(0));
    wait_vm<6 + 8 + 1>(); bar();
    wait_vm<3 + 1>(); bar();
    wait_vm<0>(); bar();
  } else {
    issue_w(0, 0); issue_x(0, 0); issue_w(1, WSLOT); issue_x(1, 32768);
    wait_vm<12>(); bar();
    issue_w(2, 2 * WSLOT); issue_x(2, 65536);
#pragma unroll 1
    for (int T = 0; T < 13; ++T) {
      wait_vm<12>();
      bar();
      issue_w(T + 3, wso); issue_x(T + 3, xso);
      wso = wnext(wso); xso = xnext(xso);
    }
    wait_vm<12>(); bar();
    wait_vm<0>(); bar();
  }
}

template <bool ISA, class PF>
DI void p1_mma(LAS char* lds, f32x16 (&acc)[2][ISA ? 3 : 2], int w, int lane, PF&& pf) {
  constexpr int NT = ISA ? 3 : 2;
  constexpr int WSLOT = ISA ? 12288 : 16384;
  constexpr int NM = 2 * NT, ND = ISA ? 8 : 4;
  const int wm = w >> 1, wn = w & 1, h = lane >> 5, rho = lane & 31;
  const int arow = wm * 64 + (ISA ? rho : (32 * ((rho >> 2) & 1) + (rho & 3) + 4 * (rho >> 3)));
  constexpr int AM = ISA ? 32 * 128 : 16 * 128;
  const int aoff = arow * 128 + ((h ^ ((arow >> 1) & 7)) * 16);
  int boff;
  if constexpr (ISA) {
    const int blk = (lane >> 4) & 1, q = (lane & 15) >> 2, p = lane & 3;
    const int c = (4 * wn + 2 * blk + (p >> 1)) ^ (((q >> 1) & 1) << 2);
    boff = P1_WOFF + (8 * h + q) * 128 + c * 16 + 8 * (p & 1);
  } else {
    const int brow = wn * 32 + rho;
    boff = P1_WOFF + brow * 128 + ((h ^ ((brow >> 1) & 7)) * 16);
  }
  LAS char* pa[2][4];
  LAS char* pb[4];
#pragma unroll
  for (int s4 = 0; s4 < 4; ++s4) {
    pa[0][s4] = lds + (aoff ^ (s4 * 32)); pa[1][s4] = pa[0][s4] + 65536;
    pb[s4] = lds + (ISA ? boff : (boff ^ (s4 * 32)));
    asm volatile("" : "+v"(pa[0][s4]), "+v"(pa[1][s4]));
    if (!ISA || s4 == 0) asm volatile("" : "+v"(pb[s4]));
  }
  bf16x8 fa0[2], fb0[NT], fa1[2], fb1[NT];
  auto rd = [&](bf16x8 (&fa)[2], bf16x8 (&fb)[NT], auto T_, auto s4_) {
    constexpr int T = decltype(T_)::value, s4 = decltype(s4_)::value;
    constexpr int xs = T % 3;
    LAS char* a = pa[xs >> 1][s4] + (xs & 1) * 32768;
    fa[0] = lds_frag(a); fa[1] = lds_frag(a + AM);
    if constexpr (ISA) {
      LAS char* b = pb[0] + ((2 * T + (s4 >> 1)) % 5) * WSLOT + (s4 & 1) * 2048;
#pragma unroll
      for (int j = 0; j < 3; ++j) fb[j] = lds_frag_tr(b + j * 4096, 512);
    } else {
      LAS char* b = pb[s4] + xs * WSLOT;
      fb[0] = lds_frag(b); fb[1] = lds_frag(b + 64 * 128);
    }
  };
  auto mm = [&](bf16x8 (&fa)[2], bf16x8 (&fb)[NT]) {
#pragma unroll
    for (int m = 0; m < 2; ++m) {
      if constexpr (ISA) {
        acc[m][0] = MFMA(fb[0], fa[m], acc[m][0]);
        acc[m][1] = MFMA(fb[1], fa[m], acc[m][1]);
        acc[m][2] = MFMA(fa[m], fb[2], acc[m][2]);
      } else {
        acc[m][0] = MFMA(fa[m], fb[0], acc[m][0]);
        acc[m][1] = MFMA(fa[m], fb[1], acc[m][1]);
      }
    }
  };
  bar();
  rd(fa0, fb0, IC(0), IC(0));
  static_for<16>([&](auto T_) {
    constexpr int T = decltype(T_)::value;
    rd(fa1, fb1, T_, IC(1)); mm(fa0, fb0); interleave<NM, ND>(); SB();
    if constexpr (ISA) { wait_lgkm0(); bar(); }
    rd(fa0, fb0, T_, IC(2)); mm(fa1, fb1); interleave<NM, ND>(); SB();
    pf(T_);
    rd(fa1, fb1, T_, IC(3)); mm(fa0, fb0); interleave<NM, ND>(); SB();
    if constexpr (T < 15) { wait_lgkm0(); bar(); rd(fa0, fb0, IC(T + 1), IC(0)); }
    mm(fa1, fb1); interleave<NM, T < 15 ? ND : 0>(); SB();
  });
}

template <bool ISA>
DI void p2_load(u32 ldsb, const char* xg0, const char* xg1, const char* yg, int lw, int lane,
                int rot, const char* flags, u64 tag, GAS u64* epoch_to_bump) {
  constexpr int NS = 4, STAGE = 32768, NK = ISA ? 16 : 32;
  const u32 lin = (u32)(lane * 16);
  u32 swE, swO;
  const char *wb0, *wb1, *hb;
  if constexpr (ISA) {
    swE = (u32)((lane >> 3) * 2048 + (((lane & 7) ^ (((lane >> 4) & 1) << 2)) * 16));
    swO = swE + 8 * 2048;
    wb0 = wb1 = yg + (lw & 1) * (32 * 2048) + (lw >> 1) * 128;
    hb = xg0 + lw * (16 * 4096);
  } else {
    swE = (u32)((lane >> 3) * 2048 + (((lane & 7) ^ (lane >> 4)) * 16));
    swO = (u32)((8 + (lane >> 3)) * 2048 + (((lane & 7) ^ (4 + (lane >> 4))) * 16));
    wb0 = xg0 + lw * (32 * 2048); wb1 = xg1 + lw * (32 * 2048);
    hb = yg + (lw >> 1) * (64 * 4096) + (lw & 1) * 4096;
  }
  auto issue_w = [&](int st, int so) {
    st = (st + rot) & (NK - 1);
    const u32 l = ldsb + so + (ISA ? 16384 : 0) + lw * 4096;
    const char* g = ISA ? wb0 + (size_t)st * (64 * 2048) : ((st & 1) ? wb1 : wb0) + (st >> 1) * 128;
    dma_s(swE, g, l); dma_s(swO, g, l + 1024); dma_s(swE, g + 32768, l + 2048); dma_s(swO, g + 32768, l + 3072);
  };
  auto issue_h = [&](int st, int so) {
    st = (st + rot) & (NK - 1);
    dma_s4_dev(lin, hb + st * (ISA ? 4096 : 8192), ldsb + so + (ISA ? 0 : 16384) + lw * 4096);
  };
#pragma unroll
  for (int s = 0; s < NS; ++s) issue_w(s, s * STAGE);
  if (lw == 0) {
    wait_flags(flags, 16, tag, lane);
    if (epoch_to_bump != nullptr && lane == 0) __hip_atomic_store(epoch_to_bump, tag, RLX_AGENT);
  }
  bar();
#pragma unroll
  for (int s = 0; s < NS; ++s) issue_h(s, s * STAGE);
  wait_vm<12>(); bar();
  int so = 0;
  auto nxt = [&]() { so = so == (NS - 1) * STAGE ? 0 : so + STAGE; };
  wait_vm<8>(); bar(); issue_h(NS + 0, so); issue_w(NS + 0, so); nxt();
  wait_vm<12>(); bar(); issue_h(NS + 1, so); issue_w(NS + 1, so); nxt();
#pragma unroll 1
  for (int t = 2; t < NK - NS; ++t) { wait_vm<16>(); bar(); issue_h(t + NS, so); issue_w(t + NS, so); nxt(); }
  wait_vm<16>(); bar(); wait_vm<8>(); bar(); wait_vm<0>(); bar();
  bar(); bar();
}

template <bool ISA>
DI void p2_mma(LAS char* lds, f32x16 (&acc)[2][2], int w, int lane) {
  constexpr int NS = 4, STAGE = 32768, NK = ISA ? 16 : 32;
  const int kh = w >> 2, wx = (w >> 1) & 1, wy = w & 1, h = lane >> 5, rho = lane & 31;
  const int blk = (lane >> 4) & 1, q = (lane & 15) >> 2, p = lane & 3;
  int xo, yo;
  if constexpr (ISA) {
    xo = wx * 8192 + kh * 2048 + h * 512 + rho * 16;
    yo = 16384 + wy * 8192 + (32 * kh + 8 * h + q) * 128 + (((2 * blk + (p >> 1)) ^ (((q >> 1) & 1) << 2)) * 16) + 8 * (p & 1);
  } else {
    const int xrow = wx * 64 + rho;
    xo = xrow * 128 + (((4 * kh + h) ^ ((xrow >> 1) & 7)) * 16);
    yo = 16384 + wy * 8192 + kh * 4096 + (2 * h * 8 + 2 * blk + (p >> 1)) * 64 + q * 16 + 8 * (p & 1);
  }
  LAS char* px[2][2];
  LAS char* py[2][2];
#pragma unroll
  for (int wdw = 0; wdw < 2; ++wdw) {
    px[wdw][0] = lds + wdw * 65536 + xo; px[wdw][1] = lds + wdw * 65536 + (xo ^ 32);
    py[wdw][0] = lds + wdw * 65536 + yo; py[wdw][1] = lds + wdw * 65536 + (yo ^ 64);
    asm volatile("" : "+v"(px[wdw][0]), "+v"(py[wdw][0]));
    if (ISA) asm volatile("" : "+v"(py[wdw][1])); else asm volatile("" : "+v"(px[wdw][1]));
  }
  bf16x8 fx0[2], fy0[2], fx1[2], fy1[2];
  auto rd = [&](bf16x8 (&fx)[2], bf16x8 (&fy)[2], auto slot_, auto step_) {
    constexpr int slot = decltype(slot_)::value, step = decltype(step_)::value;
    constexpr int wdw = slot >> 1, so = (slot & 1) * STAGE;
    if constexpr (ISA) {
      LAS char* x = px[wdw][0] + so + step * 1024;
      fx[0] = lds_frag(x); fx[1] = lds_frag(x + 4096);
      fy[0] = lds_frag_tr(py[wdw][0] + so + step * 2048, 512);
      fy[1] = lds_frag_tr(py[wdw][1] + so + step * 2048, 512);
    } else {
      LAS char* x = px[wdw][step] + so;
      fx[0] = lds_frag(x); fx[1] = lds_frag(x + 32 * 128);
      fy[0] = lds_frag_tr(py[wdw][0] + so + step * 2048, 512);
      fy[1] = lds_frag_tr(py[wdw][0] + so + step * 2048 + 256, 512);
    }
  };
  auto mm = [&](bf16x8 (&fx)[2], bf16x8 (&fy)[2]) {
#pragma unroll
    for (int i = 0; i < 2; ++i)
#pragma unroll
      for (int j = 0; j < 2; ++j)
        acc[i][j] = ISA ? MFMA(fy[j], fx[i], acc[i][j]) : MFMA(fx[i], fy[j], acc[i][j]);
  };
  bar();
  bar();
  rd(fx0, fy0, IC(0), IC(0));
  auto stage = [&](auto slot_, auto last_) {
    constexpr int slot = decltype(slot_)::value;
    constexpr bool LAST = decltype(last_)::value;
    rd(fx1, fy1, slot_, IC(1)); mm(fx0, fy0); interleave<4, 6>(); SB();
    if constexpr (!LAST) { wait_lgkm0(); bar(); rd(fx0, fy0, IC((slot + 1) % NS), IC(0)); }
    mm(fx1, fy1); interleave<4, LAST ? 0 : 6>(); SB();
  };
#pragma unroll 1
  for (int t = 0; t < NK / NS - 1; ++t) {
    stage(IC(0), std::false_type{}); stage(IC(1), std::false_type{}); stage(IC(2), std::false_type{}); stage(IC(3), std::false_type{});
  }
  stage(IC(0), std::false_type{}); stage(IC(1), std::false_type{}); stage(IC(2), std::false_type{}); stage(IC(3), std::true_type{});
}

template <bool ISA>
DI void p2_store(LAS char* lds, const f32x16 (&acc)[2][2], int w, int lane, __amdgpu_buffer_rsrc_t outr, u32 out_off, u32x2 (&xv)[8], u32x2 dsk) {
  constexpr int PITCH = 528, HALF = 128 * PITCH;
  const int kh = w >> 2, wx = (w >> 1) & 1, wy = w & 1, h = lane >> 5, rho = lane & 31;
  bar();
  LAS char* base = lds + kh * HALF + ((ISA ? wx : wy) * 64 + rho) * PITCH + ((ISA ? wy : wx) * 64 + 4 * h) * 4;
#pragma unroll
  for (int i = 0; i < 2; ++i)
#pragma unroll
    for (int j = 0; j < 2; ++j)
#pragma unroll
      for (int g = 0; g < 4; ++g) {
        const f32x4 v = {acc[i][j][4 * g], acc[i][j][4 * g + 1], acc[i][j][4 * g + 2], acc[i][j][4 * g + 3]};
        *(LAS f32x4*)(base + (ISA ? i : j) * (32 * PITCH) + ((ISA ? j : i) * 32 + 8 * g) * 4) = v;
      }
  wait_lgkm0();
  bar();
  if (!ISA) {
#pragma unroll
    for (int i = 0; i < 8; ++i) asm volatile("" : "+v"(xv[i]));
  }
  const f32x4 dv = {__uint_as_float(dsk[0] << 16), __uint_as_float(dsk[0] & 0xffff0000u), __uint_as_float(dsk[1] << 16), __uint_as_float(dsk[1] & 0xffff0000u)};
  LAS char* rp = lds + (w * 16 + h) * PITCH + rho * 16;
  const u32 vo = out_off + (u32)((w * 16 + h) * 8192 + rho * 16);
#pragma unroll
  for (int i = 0; i < 8; ++i) {
    f32x4 v = *(LAS f32x4*)(rp + 2 * i * PITCH) + *(LAS f32x4*)(rp + 2 * i * PITCH + HALF);
    if (!ISA) v += dv * (f32x4){__uint_as_float(xv[i][0] << 16), __uint_as_float(xv[i][0] & 0xffff0000u), __uint_as_float(xv[i][1] << 16), __uint_as_float(xv[i][1] & 0xffff0000u)};
    __builtin_amdgcn_raw_buffer_store_b128(__builtin_bit_cast(u32x4, v), outr, vo, i * 16384, 16);
  }
}


DI void kv_load8(u32x4 (&v)[8], __amdgpu_buffer_rsrc_t wsr, u32 vo) {
#pragma unroll
  for (int k = 0; k < 8; ++k) v[k] = __builtin_amdgcn_raw_buffer_load_b128(wsr, vo, k * (u32)KVP_BYTES, 16);
}
DI void kv_check8(u32x4 (&v)[8], __amdgpu_buffer_rsrc_t wsr, u32 vo, u32 tg) {
  const u64 t0 = __builtin_amdgcn_s_memrealtime();
  for (;;) {
    bool any = false;
#pragma unroll
    for (int k = 0; k < 8; ++k) {
      if (!__all(v[k][1] == tg && v[k][3] == tg)) {
        v[k] = __builtin_amdgcn_raw_buffer_load_b128(wsr, vo, k * (u32)KVP_BYTES, 16);
        any = true;
      }
    }
    if (!any) break;
    if (__builtin_amdgcn_s_memrealtime() - t0 > 500000ull) break;
  }
}
DI u32x2 kv_sum8(const u32x4 (&v)[8]) {
  f32x4 s0 = {0.f, 0.f, 0.f, 0.f};
#pragma unroll
  for (int k = 0; k < 8; ++k)
    s0 += (f32x4){__uint_as_float(v[k][0] << 16), __uint_as_float(v[k][0] & 0xffff0000u),
                  __uint_as_float(v[k][2] << 16), __uint_as_float(v[k][2] & 0xffff0000u)};
  return (u32x2){pk2(s0[0], s0[1]), pk2(s0[2], s0[3])};
}

DI void zero16(f32x16& v) {
#pragma unroll
  for (int i = 0; i < 16; ++i) v[i] = 0.f;
}

constexpr int LDS_WPHI = P1_WOFF + 2 * 12288;
constexpr int LDS_IMG = LDS_WPHI + 4096;
constexpr int LDS_RED = 32768;
constexpr int LDS_KVB = 0, LDS_ZB = 8192;
constexpr int LDS_CONST = 147456;
constexpr int LDS_E = LDS_CONST + 2048;
constexpr int LDS_HIN = LDS_E + 4096;

__global__ __launch_bounds__(768, 3) void fused_kernel(Params P) {
  extern __shared__ __attribute__((aligned(16))) char smem_raw[];
  LAS char* lds = (LAS char*)smem_raw;
  u64 pc;
  asm volatile("s_getpc_b64 %0" : "=s"(pc));
  const int tid = threadIdx.x, lane = tid & 63;
  const int w = __builtin_amdgcn_readfirstlane(tid >> 6);
  const int h = lane >> 5, rho = lane & 31;
  const int bid = blockIdx.x, xcd = bid & 7, idx = bid >> 3;
  const bool isA = idx < 16;
  const int li = idx & 15;
  const int c = 4 * (xcd & 1) + (li & 3), cg = 4 * (xcd >> 1) + (li >> 2);
  const int rb = 4 * (xcd & 3) + (li & 3), nb = 4 * (xcd >> 2) + (li >> 2);
  char* ws = P.ws;
  const __amdgpu_buffer_rsrc_t wsr = __builtin_amdgcn_make_buffer_rsrc(ws, 0, (int)WS_END, 0x00020000);
  const __amdgpu_buffer_rsrc_t outr = __builtin_amdgcn_make_buffer_rsrc(P.out, 0, 2048 * 2048 * 4, 0x00020000);
  GAS u64* epoch = (GAS u64*)(ws + (isA ? WS_EPOCH_A : WS_EPOCH_B));

  if (__builtin_expect(w >= 8, 1)) {
    const int lw = w - 8;
    const u32 ldsb = (u32)(size_t)lds;
    __builtin_amdgcn_s_setprio(3);
    u64 epoch_val = __hip_atomic_load(epoch, RLX_AGENT);
    if (isA) {
      const int hp = cg;
      p1_load<true>(ldsb, (const char*)(P.Ax + (size_t)c * 256 * 1024), (const char*)(P.wqkv + hp * 64), nullptr, lw, lane,
                    [&](auto) { dma_s((u32)(lane * 16), (const char*)(P.wphi + hp * 2048) + lw * 1024, ldsb + LDS_WPHI + lw * 1024); });
      asm volatile("" : "+v"(epoch_val));
      const u64 tag = next_tag(epoch_val);
      bar();
      {
        const u32 tg = (u32)tag;
        const u32 kvp0 = (u32)(WS_KVP + (size_t)(hp * 8) * KVP_BYTES);
        __builtin_amdgcn_s_setprio(0);
        __builtin_amdgcn_s_sleep(40);
        const u32 u0 = 512 + lw * 64 + lane;
        const u32 vo = kvp0 + u0 * 16;
        u32x4 v0[8], v1[8], vz[8];
        kv_load8(v0, wsr, vo); kv_load8(v1, wsr, vo + 4096);
        if (lw == 0) kv_load8(vz, wsr, vo + 8192);
        kv_check8(v0, wsr, vo, tg);
        *(LAS u32x2*)(lds + LDS_KVB + u0 * 8) = kv_sum8(v0);
        kv_check8(v1, wsr, vo + 4096, tg);
        *(LAS u32x2*)(lds + LDS_KVB + (u0 + 256) * 8) = kv_sum8(v1);
        if (lw == 0) {
          kv_check8(vz, wsr, vo + 8192, tg);
          float a = 0.f, b = 0.f;
#pragma unroll
          for (int k = 0; k < 8; ++k) { a += __uint_as_float(vz[k][0]); b += __uint_as_float(vz[k][2]); }
          const u32 hi = pk2(a, b);
          const u32 lo = pk2(a - __uint_as_float(hi << 16), b - __uint_as_float(hi & 0xffff0000u));
          const int f = 2 * (lane & 15), i4 = (lane >> 4) * 2 + (f >> 4);
          LAS char* zp = lds + LDS_ZB + i4 * 64 + ((f >> 2) & 1) * 16 + (((f >> 3) & 1) * 4 + (f & 3)) * 2;
          *(LAS u32*)zp = hi; *(LAS u32*)(zp + 32) = lo;
        }
        wait_lgkm0();
      }
      __builtin_amdgcn_s_setprio(3);
      bar(); bar();
      if (lw == 0 && lane == 0) __hip_atomic_store((GAS u64*)(ws + WS_OFLAG + c * 128 + hp * 8), tag, RLX_AGENT);
      p2_load<true>(ldsb, ws + WS_O + (size_t)rb * 4 * 16 * 4096, nullptr, (const char*)(P.wo + nb * 128), lw, lane, 2 * xcd,
                    ws + WS_OFLAG + (rb >> 1) * 128, tag, (rb == 0 && nb == 0 && lw == 0) ? epoch : nullptr);
    } else {
      const int pg = cg;
      p1_load<false>(ldsb, (const char*)(P.Bx + (size_t)c * 256 * 1024), (const char*)(P.bre + (size_t)pg * 64 * 1024),
                     (const char*)(P.bim + (size_t)pg * 64 * 1024), lw, lane, [](auto) {});
      asm volatile("" : "+v"(epoch_val));
      const u64 tag = next_tag(epoch_val);
      bar();
      if (lw == 0) {
        const f32x2 k1 = *(LAS f32x2*)(lds + LDS_CONST + lane * 32 + 16);
        const float pr = k1[0], pi = k1[1];
        const u32 tg = (u32)tag;
        GAS u64* agg = (GAS u64*)(ws + WS_AGG) + (size_t)pg * 8 * 128;
        LAS f32x2* E = (LAS f32x2*)(lds + LDS_E);
        float ar = 0.f, ai = 0.f;
#pragma unroll
        for (int k = 0; k < 8; ++k) {
          const f32x2 e = E[k * 64 + lane];
          const float a = fmaf(pr, ar, fmaf(-pi, ai, e[0])), b = fmaf(pr, ai, fmaf(pi, ar, e[1]));
          ar = a; ai = b;
        }
        __hip_atomic_store(agg + c * 128 + lane, ((u64)tg << 32) | __float_as_uint(ar), RLX_AGENT);
        __hip_atomic_store(agg + c * 128 + 64 + lane, ((u64)tg << 32) | __float_as_uint(ai), RLX_AGENT);
        float br = pr, bi = pi;
#pragma unroll
        for (int i = 0; i < 3; ++i) { const float a = br * br - bi * bi, b = 2.0f * br * bi; br = a; bi = b; }
        float vr[7], vi[7];
        const u64 t0 = __builtin_amdgcn_s_memrealtime();
        for (;;) {
          u64 x[7], y[7];
#pragma unroll
          for (int k = 0; k < 7; ++k) {
            const int kk = k < c ? k : c;
            x[k] = __hip_atomic_load(agg + kk * 128 + lane, RLX_AGENT);
            y[k] = __hip_atomic_load(agg + kk * 128 + 64 + lane, RLX_AGENT);
          }
          u32 bad = 0;
#pragma unroll
          for (int k = 0; k < 7; ++k) {
            vr[k] = __uint_as_float((u32)x[k]); vi[k] = __uint_as_float((u32)y[k]);
            bad |= k < c ? (((u32)(x[k] >> 32) ^ tg) | ((u32)(y[k] >> 32) ^ tg)) : 0u;
          }
          if (__all(bad == 0)) break;
          if (__builtin_amdgcn_s_memrealtime() - t0 > 500000ull) break;
          __builtin_amdgcn_s_sleep(1);
        }
        float hr = 0.f, hi = 0.f;
#pragma unroll
        for (int k = 0; k < 7; ++k) {
          const float a = fmaf(br, hr, fmaf(-bi, hi, vr[k])), b = fmaf(br, hi, fmaf(bi, hr, vi[k]));
          if (k < c) { hr = a; hi = b; }
        }
        *(LAS f32x2*)(lds + LDS_HIN + lane * 8) = (f32x2){hr, hi};
        wait_lgkm0();
      }
      bar(); bar();
      if (lw == 0 && lane == 0) __hip_atomic_store((GAS u64*)(ws + WS_HFLAG + c * 128 + pg * 8), tag, RLX_AGENT);
      p2_load<false>(ldsb, (const char*)(P.cre + (size_t)nb * 128 * 1024), (const char*)(P.cim + (size_t)nb * 128 * 1024),
                     ws + WS_H + (size_t)rb * 2 * 64 * 4096, lw, lane, 4 * xcd,
                     ws + WS_HFLAG + (rb >> 1) * 128, tag, (rb == 15 && nb == 0 && lw == 0) ? epoch : nullptr);
    }
    return;
  }

  const char* pfcode = (const char*)pc + (u32)(idx & 7) * (CODE_PF_LINES * 128u);
  auto pf_code = [&]() {
    if (w == 1) prefetch_lines((u32)min(lane, (int)CODE_PF_LINES - 1) << 7, pfcode, (u32)(size_t)(lds + LDS_DUMP));
  };
  pf_code();
  if (isA) {
    const int wm = w >> 1, hh = w & 1, hp = cg;
    u32 tg;
    bf16x8 phiq[2][4];
    {
      f32x16 acc[2][3];
#pragma unroll
      for (int m = 0; m < 2; ++m)
#pragma unroll
        for (int j = 0; j < 3; ++j) zero16(acc[m][j]);
      const int gb = xcd * 16 + li;
      const u32 dump = (u32)(size_t)(lds + LDS_DUMP);
      const u32 pfq = (u32)min(lane, w == 3 ? 23 : 15) << (w == 2 ? 11 : 7);
      const char* pfx = (const char*)P.Ax + (size_t)gb * (16 * 2048);
      const char* pfw = (const char*)P.wqkv + (size_t)gb * 3072;
      const char* pfo = (const char*)P.wo + (size_t)gb * 16384;
      auto pf_tile = [&](int T) {
        if (w == 2) prefetch_lines(pfq, pfx + T * 128, dump);
        if (w == 3) prefetch_lines(pfq, pfw + (size_t)T * (64 * 6144), dump);
      };
#pragma unroll
      for (int T = 3; T < PFD; ++T) pf_tile(T);
      u32 ev = __hip_atomic_load((GAS u32*)epoch, RLX_AGENT);
      p1_mma<true>(lds, acc, w, lane,
                   [&](auto T_) {
                     constexpr int T = decltype(T_)::value;
                     if constexpr (T + PFD < 16) pf_tile(T + PFD);
                     if constexpr (T >= PFT && T < PFT + 8) { if (w == 4) prefetch_lines(pfq, pfo + (T - PFT) * 2048, dump); }
                   });
      asm volatile("" : "+v"(ev));
      tg = __builtin_amdgcn_readfirstlane((u32)next_tag((u64)ev));
      const int blk = (lane >> 4) & 1, q = (lane & 15) >> 2, p = lane & 3;
      bf16x8 wph[2];
#pragma unroll
      for (int t = 0; t < 2; ++t)
        wph[t] = lds_frag_tr(lds + LDS_WPHI + hh * 2048 + (16 * t + 4 * h + q) * 64 + (16 * blk + 4 * p) * 2, 8 * 64);
      f32x16 kv[2];
      zero16(kv[0]); zero16(kv[1]);
      float z[2] = {0.f, 0.f};
      LAS char* img = lds + LDS_IMG + w * 4352;
      const u32 ones = 0x3f803f80u;
#pragma unroll
      for (int m = 0; m < 2; ++m) {
        f32x16 pp;
        u32 ep[8], em[8];
        zero16(pp);
        pp = MFMA(wph[0], pack8(acc[m][1], 0), pp);
        pp = MFMA(wph[1], pack8(acc[m][1], 1), pp);
        dual_softmax(pp, ep, em);
#pragma unroll
        for (int g = 0; g < 4; ++g) {
          const u32x2 a = {ep[2 * g], ep[2 * g + 1]}, b = {em[2 * g], em[2 * g + 1]};
          *(LAS u32x2*)(img + rho * 136 + (8 * g + 4 * h) * 2) = a;
          *(LAS u32x2*)(img + rho * 136 + (32 + 8 * g + 4 * h) * 2) = b;
        }
#pragma unroll
        for (int t = 0; t < 2; ++t) {
          const bf16x8 vf = pack8(acc[m][2], t);
#pragma unroll
          for (int F = 0; F < 2; ++F) {
            const bf16x8 af = lds_frag_tr(img + (16 * t + 4 * h + q) * 136 + (32 * F + 16 * blk + 4 * p) * 2, 8 * 136);
            kv[F] = MFMA(af, vf, kv[F]);
            const u32x4 au = __builtin_bit_cast(u32x4, af);
            asm("v_dot2c_f32_bf16 %0, %1, %5\n\tv_dot2c_f32_bf16 %0, %2, %5\n\tv_dot2c_f32_bf16 %0, %3, %5\n\tv_dot2c_f32_bf16 %0, %4, %5\n\ts_nop 2"
                : "+v"(z[F]) : "v"(au[0]), "v"(au[1]), "v"(au[2]), "v"(au[3]), "v"(ones));
          }
        }
      }
      {
        LAS char* red = lds + LDS_RED + w * 8192;
#pragma unroll
        for (int F = 0; F < 2; ++F) {
#pragma unroll
          for (int g = 0; g < 4; ++g) {
            f32x4 v = {kv[F][4 * g], kv[F][4 * g + 1], kv[F][4 * g + 2], kv[F][4 * g + 3]};
            *(LAS f32x4*)(red + ((F * 4 + g) * 64 + lane) * 16) = v;
          }
          const float zt = xhalf_sum(z[F]);
          if (h == 0) *(LAS float*)(img + (F * 32 + rho) * 4) = zt;
        }
      }
      wait_lgkm0();
      bar();
      const u32 kvp_me = (u32)(WS_KVP + (size_t)(hp * 8 + c) * KVP_BYTES);
#pragma unroll
      for (int e = 0; e < 2; ++e) {
        f32x4 s = *(LAS f32x4*)(lds + LDS_RED + e * 8192 + tid * 16);
#pragma unroll
        for (int k = 1; k < 4; ++k) s += *(LAS f32x4*)(lds + LDS_RED + (2 * k + e) * 8192 + tid * 16);
        const u32x4 g = {pk2(s[0], s[1]), tg, pk2(s[2], s[3]), tg};
        __builtin_amdgcn_raw_buffer_store_b128(g, wsr, kvp_me + (e * 512 + tid) * 16, 0, 16);
      }
      if (w == 7) {
        f32x2 s = *(LAS f32x2*)(lds + LDS_IMG + h * 4352 + rho * 8);
#pragma unroll
        for (int k = 1; k < 4; ++k) s += *(LAS f32x2*)(lds + LDS_IMG + (2 * k + h) * 4352 + rho * 8);
        const u32x4 g = {__float_as_uint(s[0]), tg, __float_as_uint(s[1]), tg};
        __builtin_amdgcn_raw_buffer_store_b128(g, wsr, kvp_me + (1024 + lane) * 16, 0, 16);
      }
#pragma unroll
      for (int m = 0; m < 2; ++m) {
        f32x16 pp;
        u32 ep[8], em[8];
        zero16(pp);
        pp = MFMA(wph[0], pack8(acc[m][0], 0), pp);
        pp = MFMA(wph[1], pack8(acc[m][0], 1), pp);
        dual_softmax(pp, ep, em);
        phiq[m][0] = frag4(ep[0], ep[1], ep[2], ep[3]); phiq[m][1] = frag4(ep[4], ep[5], ep[6], ep[7]);
        phiq[m][2] = frag4(em[0], em[1], em[2], em[3]); phiq[m][3] = frag4(em[4], em[5], em[6], em[7]);
      }
    }
    {
      const u32 kvp0 = (u32)(WS_KVP + (size_t)(hp * 8) * KVP_BYTES);
      u32x4 v[8];
      kv_load8(v, wsr, kvp0 + tid * 16);
      kv_check8(v, wsr, kvp0 + tid * 16, tg);
      *(LAS u32x2*)(lds + LDS_KVB + tid * 8) = kv_sum8(v);
      wait_lgkm0();
    }
    LAS char* kvb = lds + LDS_KVB;
    LAS char* zb = lds + LDS_ZB;
    bar();
    {
      bf16x8 kvf[4], zf[4];
#pragma unroll
      for (int i = 0; i < 4; ++i) {
        const u32x2 a = *(LAS u32x2*)(kvb + hh * 4096 + ((2 * i) * 64 + lane) * 8);
        const u32x2 b = *(LAS u32x2*)(kvb + hh * 4096 + ((2 * i + 1) * 64 + lane) * 8);
        kvf[i] = frag4(a[0], a[1], b[0], b[1]);
        zf[i] = lds_frag(zb + (hh * 4 + i) * 64 + (rho >> 4) * 32 + h * 16);
      }
#pragma unroll
      for (int m = 0; m < 2; ++m) {
        f32x16 ot, dn;
        zero16(ot); zero16(dn);
#pragma unroll
        for (int i = 0; i < 4; ++i) { dn = MFMA(zf[i], phiq[m][i], dn); ot = MFMA(kvf[i], phiq[m][i], ot); }
        const float r = __builtin_amdgcn_rcpf((dn[0] + dn[8]) + 1e-6f);
        u32 d[8];
#pragma unroll
        for (int i = 0; i < 8; ++i) d[i] = pk2(ot[2 * i] * r, ot[2 * i + 1] * r);
        const u32 obase = (u32)(WS_O + (size_t)((c * 8 + wm * 2 + m) * 16 + hp) * 4096);
#pragma unroll
        for (int i = 0; i < 2; ++i) {
          auto r0 = __builtin_amdgcn_permlane32_swap(d[4 * i], d[4 * i + 2], false, false);
          auto r1 = __builtin_amdgcn_permlane32_swap(d[4 * i + 1], d[4 * i + 3], false, false);
          u32x4 v = {r0[0], r1[0], r0[1], r1[1]};
          __builtin_amdgcn_raw_buffer_store_b128(v, wsr, obase + ((4 * hh + 2 * i + h) * 32 + rho) * 16, 0, 16);
        }
      }
    }
    wait_vm<0>();
    wait_lgkm0();
    bar();
    f32x16 acc[2][2];
    zero16(acc[0][0]); zero16(acc[0][1]); zero16(acc[1][0]); zero16(acc[1][1]);
    p2_mma<true>(lds, acc, w, lane);
    u32x2 nox[8] = {};
    p2_store<true>(lds, acc, w, lane, outr, (u32)(rb * 128 * 8192 + nb * 512), nox, nox[0]);
  } else {
    const int wm = w >> 1, pn = w & 1, pg = cg, pl = pn * 32 + rho;
    u32x2 xv[8], dsk;
    {
      f32x16 acc[2][2];
      zero16(acc[0][0]); zero16(acc[0][1]); zero16(acc[1][0]); zero16(acc[1][1]);
      const int gb = xcd * 16 + li;
      const u32 dump = (u32)(size_t)(lds + LDS_DUMP);
      const u32 pfq = (u32)min(lane, w == 4 ? 31 : 15) << (w == 4 ? 7 : 11);
      const char* pfx = (const char*)P.Bx + (size_t)gb * (16 * 2048);
      const char* pfw = (const char*)((gb & 64) ? P.bim : P.bre) + (size_t)(gb & 63) * (16 * 2048);
      const char* pfo = (const char*)((gb & 64) ? P.cim : P.cre) + (size_t)(gb & 63) * 32768;
      auto pf_tile = [&](int T) {
        if (w == 2) prefetch_lines(pfq, pfx + T * 128, dump);
        if (w == 3) prefetch_lines(pfq, pfw + T * 128, dump);
      };
#pragma unroll
      for (int T = 3; T < PFD; ++T) pf_tile(T);
      if (w == 7) {
        const int pidx = pg * 64 + lane;
        const float la = bf2f(P.lre[pidx]), lb = bf2f(P.lim[pidx]), ldt = bf2f(P.logdt[pidx]);
        const float dt = bf2f((u16)(pk2(expf(ldt), 0.f) & 0xffffu));
        const float er = expf(dt * la);
        float sn, cs;
        sincosf(dt * lb, &sn, &cs);
        const float lr = er * cs, li_ = er * sn;
        const float nr = lr - 1.0f, dn = 1.0f / (la * la + lb * lb);
        const float c2r = 2.0f * (nr * la + li_ * lb) * dn, c2i = 2.0f * (li_ * la - nr * lb) * dn;
        float pr = lr, pi = li_;
#pragma unroll
        for (int i = 0; i < 5; ++i) { const float a = pr * pr - pi * pi, b = 2.0f * pr * pi; pr = a; pi = b; }
        *(LAS f32x4*)(lds + LDS_CONST + lane * 32) = (f32x4){lr, li_, c2r, c2i};
        *(LAS f32x2*)(lds + LDS_CONST + lane * 32 + 16) = (f32x2){pr, pi};
        wait_lgkm0();
      }
      p1_mma<false>(lds, acc, w, lane,
                    [&](auto T_) {
                      constexpr int T = decltype(T_)::value;
                      if constexpr (T + PFD < 16) pf_tile(T + PFD);
                      if constexpr (T >= PFT && T < PFT + 8) { if (w == 4) prefetch_lines(pfq, pfo + (T - PFT) * 4096, dump); }
                    });
      const f32x4 k0 = *(LAS f32x4*)(lds + LDS_CONST + pl * 32);
      const f32x2 k1 = *(LAS f32x2*)(lds + LDS_CONST + pl * 32 + 16);
      const float lr = k0[0], li_ = k0[1], c2r = k0[2], c2i = k0[3], pr = k1[0], pi = k1[1];
      float gr = 0.f, gi = 0.f;
#pragma unroll
      for (int m = 0; m < 2; ++m)
#pragma unroll
        for (int r = 0; r < 16; ++r) {
          const float a = fmaf(lr, gr, fmaf(-li_, gi, acc[m][0][r]));
          const float b = fmaf(lr, gi, fmaf(li_, gr, acc[m][1][r]));
          gr = a; gi = b;
          acc[m][0][r] = a; acc[m][1][r] = b;
        }
      const int sg = 2 * wm + h;
      LAS f32x2* E = (LAS f32x2*)(lds + LDS_E);
      E[sg * 64 + pl] = (f32x2){gr, gi};
      wait_lgkm0();
      bar();
      float qr = 0.f, qi = 0.f, wr_ = 1.f, wi_ = 0.f;
#pragma unroll
      for (int k = 0; k < 7; ++k) {
        const f32x2 e = E[k * 64 + pl];
        const float a = fmaf(pr, qr, fmaf(-pi, qi, e[0])), b = fmaf(pr, qi, fmaf(pi, qr, e[1]));
        const float a2 = wr_ * pr - wi_ * pi, b2 = wr_ * pi + wi_ * pr;
        const bool take = k < sg;
        qr = take ? a : qr; qi = take ? b : qi; wr_ = take ? a2 : wr_; wi_ = take ? b2 : wi_;
      }
      f32x2 hl[32], pw[32];
      {
        const f32x2 ca = {c2r, -c2r}, cb = {-c2i, -c2i};
        f32x2 pcur = {lr, -li_};
#pragma unroll
        for (int t = 0; t < 32; ++t) {
          const f32x2 g = {acc[t >> 4][0][t & 15], acc[t >> 4][1][t & 15]};
          hl[t] = __builtin_elementwise_fma(cb, (f32x2){g[1], g[0]}, ca * g);
          pw[t] = pcur;
          pcur = __builtin_elementwise_fma((f32x2){li_, -li_}, (f32x2){pcur[1], pcur[0]}, pcur * lr);
        }
#pragma unroll
        for (int t = 0; t < 32; ++t) asm volatile("" : "+v"(hl[t]), "+v"(pw[t]));
        asm volatile("" : "+v"(qr), "+v"(qi), "+v"(wr_), "+v"(wi_));
      }
      bar();
      float cwr, cwi;
      {
        const f32x2 hin = *(LAS f32x2*)(lds + LDS_HIN + pl * 8);
        gr = fmaf(wr_, hin[0], fmaf(-wi_, hin[1], qr));
        gi = fmaf(wr_, hin[1], fmaf(wi_, hin[0], qi));
        cwr = c2r * gr - c2i * gi; cwi = c2r * gi + c2i * gr;
      }
      const u32 hbase = (u32)(WS_H + (size_t)((((c * 4 + wm) * 16 + pg) * 2) * 2 + pn) * 4096) + (u32)(((rho >> 2) * 8 + 4 * h) * 64 + (rho & 3) * 16);
#pragma unroll
      for (int tc = 0; tc < 4; ++tc) {
        f32x2 o[8];
#pragma unroll
        for (int r = 0; r < 8; ++r) {
          const f32x2 pv = pw[8 * tc + r];
          o[r] = __builtin_elementwise_fma((f32x2){cwi, -cwi}, (f32x2){pv[1], pv[0]}, __builtin_elementwise_fma((f32x2){cwr, cwr}, pv, hl[8 * tc + r]));
        }
        const u32x4 vr = {pk2(o[0][0], o[1][0]), pk2(o[2][0], o[3][0]), pk2(o[4][0], o[5][0]), pk2(o[6][0], o[7][0])};
        const u32x4 vi = {pk2(o[0][1], o[1][1]), pk2(o[2][1], o[3][1]), pk2(o[4][1], o[5][1]), pk2(o[6][1], o[7][1])};
        __builtin_amdgcn_raw_buffer_store_b128(vr, wsr, hbase + tc * 64, 0, 16);
        __builtin_amdgcn_raw_buffer_store_b128(vi, wsr, hbase + tc * 64 + 8192, 0, 16);
      }
    }
    wait_vm<0>();
    wait_lgkm0();
    bar();
    {
      const u16* xp = P.Bx + (size_t)(rb * 128 + w * 16 + h) * 1024 + nb * 128 + rho * 4;
#pragma unroll
      for (int i = 0; i < 8; ++i) xv[i] = *(const u32x2*)(xp + 2 * i * 1024);
      dsk = *(const u32x2*)(P.dskip + nb * 128 + rho * 4);
    }
    f32x16 acc[2][2];
    zero16(acc[0][0]); zero16(acc[0][1]); zero16(acc[1][0]); zero16(acc[1][1]);
    p2_mma<false>(lds, acc, w, lane);
    p2_store<false>(lds, acc, w, lane, outr, (u32)(rb * 128 * 8192 + 4096 + nb * 512), xv, dsk);
  }
}


extern "C" void kernel_launch(void* const* d_in, const int* in_sizes, int n_in,
                              void* d_out, int out_size, void* d_ws, size_t ws_size,
                              hipStream_t stream) {
  Params P;
  P.Ax = (const u16*)d_in[0]; P.wqkv = (const u16*)d_in[1]; P.wphi = (const u16*)d_in[2]; P.wo = (const u16*)d_in[3];
  P.Bx = (const u16*)d_in[4]; P.lre = (const u16*)d_in[5]; P.lim = (const u16*)d_in[6];
  P.bre = (const u16*)d_in[7]; P.bim = (const u16*)d_in[8]; P.cre = (const u16*)d_in[9]; P.cim = (const u16*)d_in[10];
  P.logdt = (const u16*)d_in[11]; P.dskip = (const u16*)d_in[12];
  P.out = (float*)d_out; P.ws = (char*)d_ws;
  (void)hipFuncSetAttribute((const void*)fused_kernel, hipFuncAttributeMaxDynamicSharedMemorySize, LDS_BYTES);
  fused_kernel<<<256, 768, LDS_BYTES, stream>>>(P);
}
```
